```python
import torch

def _softmax(x, axis=-1):
    return torch.softmax(x, dim=axis)
def _gelu(x):
    return torch.nn.functional.gelu(x, approximate="tanh")
def _silu(x):
    return torch.nn.functional.silu(x)
def _sigmoid(x):
    return torch.sigmoid(x)
def _layernorm(x, axis=-1, eps=1e-5):
    m = x.mean(dim=axis, keepdim=True)
    v = x.var(dim=axis, keepdim=True, unbiased=False)
    return (x - m) / torch.sqrt(v + eps)
def _scatter_add(tgt, idx, src, axis=0):
    return tgt.clone().index_add_(axis, idx, src)
def _segment_sum(src, seg, n):
    out = torch.zeros((n,) + src.shape[1:], dtype=src.dtype)
    return out.index_add_(0, seg, src)
def _pad(x, pw):
    flat = tuple(v for pair in reversed(pw) for v in pair)
    return torch.nn.functional.pad(x, flat)

kernel_name = 'recurrent_scan__grouped_kv_mqa__glu_gated_swiglu__prompt'

def setup_inputs():

    _w = (lambda a: (lambda t: t.to(torch.bfloat16) if t.is_floating_point() else t)(torch.as_tensor(a)))
    def _A_setup():
        B, S, D, H = 1, 2048, 576, 32
        Dh = D // H
        return {
            "x": _w((lambda *s: torch.randn(*s, dtype=torch.bfloat16) * 0.05)(B, S, D)),
            "wq": _w((lambda *s: torch.randn(*s, dtype=torch.bfloat16) * 0.2)(D, H * Dh)),
            "wk": _w((lambda *s: torch.randn(*s, dtype=torch.bfloat16) * 0.2)(D, Dh)),
            "wv": _w((lambda *s: torch.randn(*s, dtype=torch.bfloat16) * 0.2)(D, Dh)),
            "wo": _w((lambda *s: torch.randn(*s, dtype=torch.bfloat16) * 0.2)(H * Dh, D)),
        }
    _A_in = _A_setup()

    def _B_setup():
        B, S, D, F = 1, 2048, 576, 4608
        return {
            "x": _w((lambda *s: torch.randn(*s, dtype=torch.bfloat16) * 0.05)(B, S, D)),
            "w1": _w((lambda *s: torch.randn(*s, dtype=torch.bfloat16) * 0.1)(D, F) * (2.0 / D) ** 0.5),
            "w3": _w((lambda *s: torch.randn(*s, dtype=torch.bfloat16) * 0.1)(D, F) * (2.0 / D) ** 0.5),
            "w2": _w((lambda *s: torch.randn(*s, dtype=torch.bfloat16) * 0.1)(F, D) * (2.0 / F) ** 0.5),
        }
    _B_in = _B_setup()

    return {
        "A_x": _A_in["x"],
        "A_wq": _A_in["wq"],
        "A_wk": _A_in["wk"],
        "A_wv": _A_in["wv"],
        "A_wo": _A_in["wo"],
        "B_x": _B_in["x"],
        "B_w1": _B_in["w1"],
        "B_w3": _B_in["w3"],
        "B_w2": _B_in["w2"],
    }

def reference(A_x, A_wq, A_wk, A_wv, A_wo, B_x, B_w1, B_w3, B_w2):
    def _A_ref(x, wq, wk, wv, wo):
        B, S, D = x.shape
        H = 32
        Dh = D // H
        q = (lambda x, *d: x.permute(*d))((x @ wq).reshape(B, S, H, Dh), 0, 2, 1, 3)
        k = x @ wk
        v = x @ wv
        scores = torch.einsum("bhqd,bkd->bhqk", q, k) * (Dh ** -0.5)
        p = _softmax(scores, axis=-1)
        o = torch.einsum("bhqk,bkd->bhqd", p, v)
        return (lambda x, *d: x.permute(*d))(o, 0, 2, 1, 3).reshape(B, S, H * Dh) @ wo

    def _B_ref(x, w1, w3, w2):
        gate = torch.einsum("bsd,df->bsf", x, w1)
        up = torch.einsum("bsd,df->bsf", x, w3)
        h = _silu(gate) * up
        out = torch.einsum("bsf,fd->bsd", h, w2)
        return out

    x = A_x
    xn = _layernorm(x, axis=-1)
    s = _A_ref(xn, A_wq, A_wk, A_wv, A_wo)
    g = _silu(_B_ref(xn, B_w1, B_w3, B_w2))
    return x + s * g
```

```cpp
#include <hip/hip_runtime.h>
#include <hip/hip_bf16.h>
#include <stdint.h>

typedef unsigned short u16;
using bf16x8 = __attribute__((ext_vector_type(8))) short;
using f32x4 = __attribute__((ext_vector_type(4))) float;
using f32x16 = __attribute__((ext_vector_type(16))) float;

constexpr int S = 2048, D = 576, H = 32, DH = 18, F = 4608;

__device__ __forceinline__ float bf2f(u16 v) { return __uint_as_float(((unsigned)v) << 16); }
__device__ __forceinline__ u16 f2bf(float x) { unsigned u = __float_as_uint(x); u += 0x7fffu + ((u >> 16) & 1u); return (u16)(u >> 16); }

__global__ __launch_bounds__(256) void k_ln(const u16* __restrict__ x, u16* __restrict__ xn) {
  int row = blockIdx.x * 4 + (threadIdx.x >> 6), lane = threadIdx.x & 63;
  float v[9]; float s = 0;
  for (int i = 0; i < 9; ++i) { v[i] = bf2f(x[row * D + i * 64 + lane]); s += v[i]; }
  for (int o = 32; o; o >>= 1) s += __shfl_xor(s, o);
  float m = s * (1.f / D); float q = 0;
  for (int i = 0; i < 9; ++i) { float d = v[i] - m; q += d * d; }
  for (int o = 32; o; o >>= 1) q += __shfl_xor(q, o);
  float r = 1.f / sqrtf(q * (1.f / D) + 1e-5f);
  for (int i = 0; i < 9; ++i) xn[row * D + i * 64 + lane] = f2bf((v[i] - m) * r);
}

template <int MODE>
__global__ __launch_bounds__(256) void k_gemm(const u16* __restrict__ A, int lda, const u16* __restrict__ B, int ldb, u16* __restrict__ C, int ldc, int M, int N, int K) {
  __shared__ u16 As[64][16 + 8];
  __shared__ u16 Bs[64][16 + 8];
  int tid = threadIdx.x, wid = tid >> 6, lane = tid & 63;
  int m0 = blockIdx.y * 64, n0 = blockIdx.x * 64;
  int wm = wid >> 1, wn = wid & 1;
  f32x16 acc = {};
  for (int k0 = 0; k0 < K; k0 += 16) {
    for (int i = tid; i < 64 * 16; i += 256) { int r = i >> 4, c = i & 15; As[r][c] = (m0 + r < M && k0 + c < K) ? A[(size_t)(m0 + r) * lda + k0 + c] : (u16)0; }
    for (int i = tid; i < 64 * 16; i += 256) { int c = i >> 6, n = i & 63; Bs[n][c] = (n0 + n < N && k0 + c < K) ? B[(size_t)(k0 + c) * ldb + n0 + n] : (u16)0; }
    __syncthreads();
    bf16x8 a, b;
    for (int j = 0; j < 8; ++j) { a[j] = (short)As[wm * 32 + (lane & 31)][8 * (lane >> 5) + j]; b[j] = (short)Bs[wn * 32 + (lane & 31)][8 * (lane >> 5) + j]; }
    acc = __builtin_amdgcn_mfma_f32_32x32x16_bf16(a, b, acc, 0, 0, 0);
    __syncthreads();
  }
  for (int r = 0; r < 16; ++r) {
    int row = m0 + wm * 32 + (r & 3) + 8 * (r >> 2) + 4 * (lane >> 5), col = n0 + wn * 32 + (lane & 31);
    if (row < M && col < N) C[(size_t)row * ldc + col] = f2bf(acc[r]);
  }
}

__global__ __launch_bounds__(256) void k_attn(const u16* __restrict__ q, const u16* __restrict__ k, const u16* __restrict__ v, u16* __restrict__ o) {
  __shared__ float Ks[256][DH + 1];
  __shared__ float Vs[256][DH + 1];
  int gid = blockIdx.x * 256 + threadIdx.x;
  int row = gid >> 5, h = gid & 31;
  float qv[DH], acc[DH];
  for (int d = 0; d < DH; ++d) { qv[d] = bf2f(q[row * D + h * DH + d]) * (0.23570226039551584f * 1.4426950408889634f); acc[d] = 0; }
  float m = -1e30f, l = 0;
  for (int k0 = 0; k0 < S; k0 += 256) {
    __syncthreads();
    for (int i = threadIdx.x; i < 256 * DH; i += 256) { int r = i / DH, c = i % DH; Ks[r][c] = bf2f(k[(k0 + r) * DH + c]); Vs[r][c] = bf2f(v[(k0 + r) * DH + c]); }
    __syncthreads();
    for (int j = 0; j < 256; ++j) {
      float s = 0;
      for (int d = 0; d < DH; ++d) s += qv[d] * Ks[j][d];
      float mn = fmaxf(m, s);
      float a = exp2f(m - mn), p = exp2f(s - mn);
      l = l * a + p;
      for (int d = 0; d < DH; ++d) acc[d] = acc[d] * a + p * Vs[j][d];
      m = mn;
    }
  }
  for (int d = 0; d < DH; ++d) o[row * D + h * DH + d] = f2bf(acc[d] / l);
}

__global__ void k_silu_mul(const u16* __restrict__ g, const u16* __restrict__ u, u16* __restrict__ h, int n) {
  int i = blockIdx.x * 256 + threadIdx.x;
  if (i < n) { float a = bf2f(g[i]); float s = a / (1.f + __expf(-a)); h[i] = f2bf(bf2f(f2bf(s)) * bf2f(u[i])); }
}
__global__ void k_final(const u16* __restrict__ x, const u16* __restrict__ s, const u16* __restrict__ f, u16* __restrict__ out, int n) {
  int i = blockIdx.x * 256 + threadIdx.x;
  if (i < n) { float a = bf2f(f[i]); float g = a / (1.f + __expf(-a)); out[i] = f2bf(bf2f(x[i]) + bf2f(s[i]) * g); }
}

extern "C" void kernel_launch(void* const* d_in, const int* in_sizes, int n_in, void* d_out, int out_size, void* d_ws, size_t ws_size, hipStream_t stream) {
  const u16* x = (const u16*)d_in[0];
  const u16* wq = (const u16*)d_in[1];
  const u16* wk = (const u16*)d_in[2];
  const u16* wv = (const u16*)d_in[3];
  const u16* wo = (const u16*)d_in[4];
  const u16* w1 = (const u16*)d_in[6];
  const u16* w3 = (const u16*)d_in[7];
  const u16* w2 = (const u16*)d_in[8];
  u16* out = (u16*)d_out;
  u16* ws = (u16*)d_ws;
  u16* xn = ws;
  u16* q = xn + S * D;
  u16* k = q + S * D;
  u16* v = k + S * 32;
  u16* o = v + S * 32;
  u16* sres = o + S * D;
  u16* gate = sres + S * D;
  u16* up = gate + S * F;
  u16* hbuf = up + S * F;
  u16* ffn = hbuf + S * F;
  k_ln<<<S / 4, 256, 0, stream>>>(x, xn);
  k_gemm<0><<<dim3(D / 64, S / 64), 256, 0, stream>>>(xn, D, wq, D, q, D, S, D, D);
  k_gemm<0><<<dim3(1, S / 64), 256, 0, stream>>>(xn, D, wk, DH, k, DH, S, DH, D);
  k_gemm<0><<<dim3(1, S / 64), 256, 0, stream>>>(xn, D, wv, DH, v, DH, S, DH, D);
  k_attn<<<S * H / 256, 256, 0, stream>>>(q, k, v, o);
  k_gemm<0><<<dim3(D / 64, S / 64), 256, 0, stream>>>(o, D, wo, D, sres, D, S, D, D);
  k_gemm<0><<<dim3(F / 64, S / 64), 256, 0, stream>>>(xn, D, w1, F, gate, F, S, F, D);
  k_gemm<0><<<dim3(F / 64, S / 64), 256, 0, stream>>>(xn, D, w3, F, up, F, S, F, D);
  k_silu_mul<<<S * F / 256, 256, 0, stream>>>(gate, up, hbuf, S * F);
  k_gemm<0><<<dim3(D / 64, S / 64), 256, 0, stream>>>(hbuf, F, w2, D, ffn, D, S, D, F);
  k_final<<<S * D / 256, 256, 0, stream>>>(x, sres, ffn, out, S * D);
}
```

```cpp
#include <hip/hip_runtime.h>
#include <hip/hip_bf16.h>
#include <stdint.h>

typedef unsigned short u16;
using bf16x8 = __attribute__((ext_vector_type(8))) short;
using v4s = __attribute__((ext_vector_type(4))) short;
using f32x4 = __attribute__((ext_vector_type(4))) float;
using f32x16 = __attribute__((ext_vector_type(16))) float;
using u32x4 = __attribute__((ext_vector_type(4))) unsigned;
using u32x2 = __attribute__((ext_vector_type(2))) unsigned;
#define DI __device__ __forceinline__
#define LAS __attribute__((address_space(3)))
#define MFMA32(a, b, c) __builtin_amdgcn_mfma_f32_32x32x16_bf16((a), (b), (c), 0, 0, 0)
#define SCHED() __builtin_amdgcn_sched_barrier(0)

constexpr int S = 2048, D = 576, H = 32, DH = 18, F = 4608;
constexpr float QSCALE = 0.23570226039551584f * 1.4426950408889634f;
constexpr float LOG2E = 1.4426950408889634f;

DI float bf2f(u16 v) { return __uint_as_float(((unsigned)v) << 16); }
DI u16 f2bf(float x) { unsigned u = __float_as_uint(x); u += 0x7fffu + ((u >> 16) & 1u); return (u16)(u >> 16); }
using f32x2 = __attribute__((ext_vector_type(2))) float;
using bf2 = __attribute__((ext_vector_type(2))) __bf16;
DI unsigned cvtpk(float lo, float hi) { f32x2 v = {lo, hi}; return __builtin_bit_cast(unsigned, __builtin_convertvector(v, bf2)); }
DI float silu(float a) { return a * __builtin_amdgcn_rcpf(1.f + __builtin_amdgcn_exp2f(-LOG2E * a)); }

#define TDECL
#define TSTAMP(i)
#define RSTAMP(i)
#define TFLUSH()

#define MFMA16(a, b, c) __builtin_amdgcn_mfma_f32_16x16x32_bf16((a), (b), (c), 0, 0, 0)
typedef __attribute__((ext_vector_type(4))) short v4i16_t;
DI bf16x8 trread(LAS char* p, int imm0, int imm1) {
  v4i16_t a = __builtin_amdgcn_ds_read_tr16_b64_v4i16((LAS v4i16_t*)(p + imm0));
  v4i16_t b = __builtin_amdgcn_ds_read_tr16_b64_v4i16((LAS v4i16_t*)(p + imm1));
  return __builtin_shufflevector(a, b, 0, 1, 2, 3, 4, 5, 6, 7);
}
DI void glds(unsigned voff, unsigned ldsw, int imm, const char* g) {
  asm volatile("s_add_u32 m0, %1, %2\n\ts_nop 0\n\tglobal_load_lds_dwordx4 %0, %3" ::"v"(voff), "s"(ldsw), "i"(imm), "s"(g) : "memory", "scc");
}
template <int OFF>
DI void gload(u32x4& f, unsigned voff, const char* g) {
  asm volatile("global_load_dwordx4 %0, %1, %2 offset:%3" : "=v"(f) : "v"(voff), "s"(g), "i"(OFF) : "memory");
}
using u32x4u = __attribute__((ext_vector_type(4), aligned(4))) unsigned;
constexpr int PRE_LDS = 576 * 160;
template <bool LAST>
DI void pre_body(LAS char* lds, const u16* __restrict__ x, const u16* __restrict__ wq, const u16* __restrict__ wk, const u16* __restrict__ wv,
                 u16* __restrict__ xnp, u16* __restrict__ q, u16* __restrict__ kvf, int tg, int cg, int wid, int lane) {
  const int t16 = lane & 15, g = lane >> 4;
  const int row = tg * 64 + wid * 16 + t16;
  u32x4 X[18];
  {
    const unsigned xoff = (unsigned)(row * (D * 2) + g * 16);
    const char* xb = (const char*)x;
#define XL(kk) gload<(kk) * 64>(X[kk], xoff, xb)
    XL(0); XL(1); XL(2); XL(3); XL(4); XL(5); XL(6); XL(7); XL(8); XL(9); XL(10); XL(11); XL(12); XL(13); XL(14); XL(15); XL(16); XL(17);
#undef XL
  }
  if (!LAST) {
    const unsigned ldsw = __builtin_amdgcn_readfirstlane((unsigned)(size_t)lds + wid * 1024);
    unsigned vo[5];
#pragma unroll
    for (int i = 0; i < 5; ++i) {
      const int c = 256 * i + 64 * wid + lane, r = c / 10, c16 = c - r * 10;
      const int k = (r & ~31) + 8 * ((r >> 2) & 3) + 4 * ((r >> 4) & 1) + (r & 3);
      vo[i] = (unsigned)(k * (D * 2) + c16 * 16);
    }
    const char* wb = (const char*)wq + cg * 160;
#pragma unroll
    for (int i = 0; i < 23; ++i) {
      if (i < 22) glds(vo[i % 5], ldsw, i * 4096, wb + (i / 5) * (128 * D * 2));
      else if (wid < 2) glds(vo[i % 5], ldsw, i * 4096, wb + (i / 5) * (128 * D * 2));
    }
  } else {
#pragma unroll
    for (int it = 0; it < 3; ++it) {
      const int k = it * 256 + wid * 64 + lane;
      if (k < D) {
        const u32x4 q0 = *(const u32x4*)(wq + (size_t)k * D + 560), q1 = *(const u32x4*)(wq + (size_t)k * D + 568);
        const u32x4 k0 = *(const u32x4u*)(wk + k * DH), k1 = *(const u32x4u*)(wk + k * DH + 8);
        const u32x4 v0 = *(const u32x4u*)(wv + k * DH), v1 = *(const u32x4u*)(wv + k * DH + 8);
        const unsigned k2 = *(const unsigned*)(wk + k * DH + 16), v2 = *(const unsigned*)(wv + k * DH + 16);
        const int r = (k & ~31) + 16 * ((k >> 2) & 1) + 4 * ((k >> 3) & 3) + (k & 3);
        LAS char* d = lds + r * 160;
        *(LAS u32x4*)(d) = q0; *(LAS u32x4*)(d + 16) = q1; *(LAS u32x4*)(d + 32) = k0; *(LAS u32x4*)(d + 48) = k1;
        *(LAS u32x4*)(d + 64) = v0; *(LAS u32x4*)(d + 80) = v1; *(LAS u32x2*)(d + 96) = u32x2{v2, k2};
      }
    }
  }
  if (!LAST) {
    asm volatile("s_waitcnt vmcnt(22)" : "+v"(X[0]), "+v"(X[1]), "+v"(X[2]), "+v"(X[3]), "+v"(X[4]), "+v"(X[5]), "+v"(X[6]), "+v"(X[7]), "+v"(X[8]) :: "memory");
    asm volatile("" : "+v"(X[9]), "+v"(X[10]), "+v"(X[11]), "+v"(X[12]), "+v"(X[13]), "+v"(X[14]), "+v"(X[15]), "+v"(X[16]), "+v"(X[17]) :: "memory");
  } else {
    asm volatile("s_waitcnt vmcnt(0)" : "+v"(X[0]), "+v"(X[1]), "+v"(X[2]), "+v"(X[3]), "+v"(X[4]), "+v"(X[5]), "+v"(X[6]), "+v"(X[7]), "+v"(X[8]) :: "memory");
    asm volatile("" : "+v"(X[9]), "+v"(X[10]), "+v"(X[11]), "+v"(X[12]), "+v"(X[13]), "+v"(X[14]), "+v"(X[15]), "+v"(X[16]), "+v"(X[17]) :: "memory");
  }
  const u32x4 onesu = {0x3F803F80u, 0x3F803F80u, 0x3F803F80u, 0x3F803F80u};
  const bf16x8 ones = __builtin_bit_cast(bf16x8, onesu);
  f32x4 s4 = {}, g4 = {};
#pragma unroll
  for (int kk = 0; kk < 18; ++kk) {
    const bf16x8 xf = __builtin_bit_cast(bf16x8, X[kk]);
    s4 = MFMA16(ones, xf, s4);
    g4 = MFMA16(xf, xf, g4);
  }
  const int l3 = lane & 3;
  const float dsel = l3 == 0 ? g4[0] : (l3 == 1 ? g4[1] : (l3 == 2 ? g4[2] : g4[3]));
  const float ss = __shfl(dsel, t16 + 16 * (t16 >> 2));
  const float mean = s4[0] * (1.f / D);
  const float var = fmaxf(ss * (1.f / D) - mean * mean, 0.f);
  const float rstd = __builtin_amdgcn_rsqf(var + 1e-5f);
  const float nb = -mean * rstd;
#pragma unroll
  for (int kk = 0; kk < 18; ++kk) {
#pragma unroll
    for (int i = 0; i < 4; ++i) {
      const unsigned w = X[kk][i];
      X[kk][i] = cvtpk(fmaf(__uint_as_float(w << 16), rstd, nb), fmaf(__uint_as_float(w & 0xffff0000u), rstd, nb));
    }
  }
  asm volatile("" : "+v"(X[0]), "+v"(X[1]), "+v"(X[2]), "+v"(X[3]), "+v"(X[4]), "+v"(X[5]), "+v"(X[6]), "+v"(X[7]), "+v"(X[8]) :: "memory");
  asm volatile("" : "+v"(X[9]), "+v"(X[10]), "+v"(X[11]), "+v"(X[12]), "+v"(X[13]), "+v"(X[14]), "+v"(X[15]), "+v"(X[16]), "+v"(X[17]) :: "memory");
  f32x4 acc[5];
#pragma unroll
  for (int b = 0; b < 5; ++b) acc[b] = f32x4{};
  LAS char* trb = lds + (4 * g + (t16 >> 2)) * 160 + 8 * (lane & 3);
#pragma unroll
  for (int half = 0; half < 2; ++half) {
    if (!LAST) { if (half == 0) asm volatile("s_waitcnt vmcnt(11)" ::: "memory"); else asm volatile("s_waitcnt vmcnt(0)" ::: "memory"); }
    if (!LAST || half == 0) { asm volatile("s_waitcnt lgkmcnt(0)" ::: "memory"); __builtin_amdgcn_s_barrier(); }
    SCHED();
#pragma unroll
    for (int k9 = 0; k9 < 9; ++k9) {
      const int kk = half * 9 + k9;
      const bf16x8 xf = __builtin_bit_cast(bf16x8, X[kk]);
      if (!LAST) {
#pragma unroll
        for (int b = 0; b < 5; ++b) acc[b] = MFMA16(trread(trb, kk * 5120 + 32 * b, kk * 5120 + 2560 + 32 * b), xf, acc[b]);
      } else {
        acc[0] = MFMA16(trread(trb, kk * 5120, kk * 5120 + 2560), xf, acc[0]);
        acc[1] = MFMA16(trread(trb, kk * 5120 + 32, kk * 5120 + 2560 + 32), xf, acc[1]);
        acc[2] = MFMA16(xf, trread(trb, kk * 5120 + 64, kk * 5120 + 2560 + 64), acc[2]);
        const bf16x8 w3 = trread(trb, kk * 5120 + 96, kk * 5120 + 2560 + 96);
        acc[3] = MFMA16(xf, w3, acc[3]);
        acc[4] = MFMA16(w3, xf, acc[4]);
      }
    }
  }
  {
    const int tb = tg * 2 + (wid >> 1);
    char* xd = (char*)xnp + ((size_t)(tb * 36 + (g >> 1)) * 64 + (g & 1) * 32 + 16 * (wid & 1) + t16) * 16;
#pragma unroll
    for (int kk = 0; kk < 18; ++kk) if ((kk & 7) == cg) *(u32x4*)(xd + kk * 2048) = X[kk];
  }
  if (!LAST) {
    u16* qd = q + (size_t)row * D + cg * 80 + 4 * g;
#pragma unroll
    for (int b = 0; b < 5; ++b) *(u32x2*)(qd + 16 * b) = u32x2{cvtpk(acc[b][0] * QSCALE, acc[b][1] * QSCALE), cvtpk(acc[b][2] * QSCALE, acc[b][3] * QSCALE)};
  } else {
    *(u32x2*)(q + (size_t)row * D + 560 + 4 * g) = u32x2{cvtpk(acc[0][0] * QSCALE, acc[0][1] * QSCALE), cvtpk(acc[0][2] * QSCALE, acc[0][3] * QSCALE)};
    const int tile = tg * 2 + (wid >> 1), s = wid & 1, key = 16 * s + t16;
    char* kb = (char*)kvf + (size_t)tile * 4096;
    *(u32x2*)(kb + ((g >> 1) * 32 + key) * 16 + (g & 1) * 8) = u32x2{cvtpk(acc[1][0], acc[1][1]), cvtpk(acc[1][2], acc[1][3])};
    char* vb = kb + (2 + s) * 1024 + (g & 1) * 512 + (g >> 1) * 8;
    *(u32x2*)(vb + t16 * 16) = u32x2{cvtpk(acc[2][0], acc[2][1]), cvtpk(acc[2][2], acc[2][3])};
    const unsigned cst = (t16 == 2 || t16 == 4) ? 0x3F803F80u : 0u;
    u32x2 e = t16 < 2 ? u32x2{cvtpk(acc[3][0], acc[3][1]), cvtpk(acc[3][2], acc[3][3])} : u32x2{cst, cst};
    *(u32x2*)(vb + (16 + t16) * 16) = e;
    if (g < 2) {
      u32x4 k1 = g == 0 ? u32x4{cvtpk(acc[4][2], acc[4][3]), 0x00003F80u, 0u, 0u} : u32x4{0u, 0u, 0u, 0u};
      *(u32x4*)(kb + 1024 + (g * 32 + key) * 16) = k1;
    }
  }
}
__global__ __launch_bounds__(256) void k_pre(const u16* __restrict__ x, const u16* __restrict__ wq, const u16* __restrict__ wk, const u16* __restrict__ wv,
                                             u16* __restrict__ xnp, u16* __restrict__ q, u16* __restrict__ kvf) {
  __shared__ __attribute__((aligned(16))) char smem[PRE_LDS];
  const int wid = __builtin_amdgcn_readfirstlane(threadIdx.x >> 6), lane = threadIdx.x & 63;
  const int xcd = blockIdx.x & 7, idx = blockIdx.x >> 3;
  const int tg = xcd * 4 + (idx >> 3), cg = idx & 7;
  if (cg < 7) pre_body<false>((LAS char*)smem, x, wq, wk, wv, xnp, q, kvf, tg, cg, wid, lane);
  else pre_body<true>((LAS char*)smem, x, wq, wk, wv, xnp, q, kvf, tg, cg, wid, lane);
}

constexpr float MSHIFT = 80.f;
DI float xhalf_max(float v) { auto rr = __builtin_amdgcn_permlane32_swap(__float_as_uint(v), __float_as_uint(v), false, false); return fmaxf(__uint_as_float(rr[0]), __uint_as_float(rr[1])); }
DI float max16(const f32x16& s) {
  float a = fmaxf(fmaxf(s[0], s[1]), s[2]), b = fmaxf(fmaxf(s[3], s[4]), s[5]);
  a = fmaxf(fmaxf(a, s[6]), s[7]); b = fmaxf(fmaxf(b, s[8]), s[9]);
  a = fmaxf(fmaxf(a, s[10]), s[11]); b = fmaxf(fmaxf(b, s[12]), s[13]);
  a = fmaxf(fmaxf(a, s[14]), s[15]); return fmaxf(a, b);
}
DI void exp_pack(const f32x16& s, bf16x8& p0, bf16x8& p1) {
  u32x4 a, b;
#pragma unroll
  for (int i = 0; i < 4; ++i) a[i] = cvtpk(__builtin_amdgcn_exp2f(s[2 * i]), __builtin_amdgcn_exp2f(s[2 * i + 1]));
#pragma unroll
  for (int i = 0; i < 4; ++i) b[i] = cvtpk(__builtin_amdgcn_exp2f(s[8 + 2 * i]), __builtin_amdgcn_exp2f(s[8 + 2 * i + 1]));
  p0 = __builtin_bit_cast(bf16x8, a); p1 = __builtin_bit_cast(bf16x8, b);
}
DI void store_o(u16* __restrict__ op, int row, int h, int hi, const float* acc  , float rl) {
  const int tb = row >> 5, tok = row & 31;
  u32x4 a = {cvtpk(acc[0] * rl, acc[1] * rl), cvtpk(acc[2] * rl, acc[3] * rl), cvtpk(acc[4] * rl, acc[5] * rl), cvtpk(acc[6] * rl, acc[7] * rl)};
  *(u32x4*)(op + ((size_t)(tb * 36 + h) * 64 + hi * 32 + tok) * 8) = a;
  if (!hi) {
    const int hl = h & 7;
    *(unsigned*)(op + ((size_t)(tb * 36 + 32 + (h >> 3)) * 64 + ((hl >> 1) & 1) * 32 + tok) * 8 + (2 * (hl >> 2) + (hl & 1)) * 2) = cvtpk(acc[8] * rl, acc[9] * rl);
  }
}
template <int T, int NSPLIT, int RG>
__global__ __launch_bounds__(64 * NSPLIT * RG) void k_attn(const u16* __restrict__ q, const u16* __restrict__ kvf, u16* __restrict__ op) {
  constexpr int NT = 64 / NSPLIT;
  __shared__ float red[RG * NSPLIT * T * 12 * 64];
  const int wid = threadIdx.x >> 6, lane = threadIdx.x & 63, hi = lane >> 5, h = lane & 31;
  const int rg = wid / NSPLIT, sp = wid % NSPLIT;
  const int row0 = (blockIdx.x * RG + rg) * T;
  bf16x8 Q0[T], Q1[T];
#pragma unroll
  for (int t = 0; t < T; ++t) {
    const u16* qp = q + (size_t)(row0 + t) * D + h * DH;
    Q0[t] = *(const bf16x8*)(qp + 8 * hi);
    unsigned w = *(const unsigned*)(qp + 16);
    u32x4 x = {hi ? 0u : w, 0u, 0u, 0u};
    Q1[t] = __builtin_bit_cast(bf16x8, x);
  }
  const bf16x8* kv = (const bf16x8*)kvf + (size_t)sp * NT * 256 + lane;
  bf16x8 K0 = kv[0], K1 = kv[64], V0 = kv[128], V1 = kv[192];
  float mref[T];
  const f32x16 zero = {};
#pragma unroll
  for (int t = 0; t < T; ++t) {
    f32x16 s = MFMA32(K0, Q0[t], zero); s = MFMA32(K1, Q1[t], s);
    float m = xhalf_max(max16(s)) + MSHIFT;
    unsigned mb = (unsigned)f2bf(-m);
    mref[t] = -bf2f((u16)mb);
    u32x4 x = __builtin_bit_cast(u32x4, Q1[t]); x[1] = hi ? 0u : mb; Q1[t] = __builtin_bit_cast(bf16x8, x);
  }
  f32x16 O[T];
#pragma unroll
  for (int t = 0; t < T; ++t) O[t] = zero;
#define TILE_STEP(K0_, K1_, V0_, V1_)                                   \
  _Pragma("unroll") for (int t = 0; t < T; ++t) {                          \
    f32x16 s = MFMA32(K0_, Q0[t], zero); s = MFMA32(K1_, Q1[t], s);        \
    bf16x8 p0, p1; exp_pack(s, p0, p1);                                    \
    O[t] = MFMA32(V0_, p0, O[t]); O[t] = MFMA32(V1_, p1, O[t]);            \
  }
  for (int it = 0; it < NT; it += 2) {
    bf16x8 nK0 = kv[(it + 1) * 256], nK1 = kv[(it + 1) * 256 + 64], nV0 = kv[(it + 1) * 256 + 128], nV1 = kv[(it + 1) * 256 + 192];
    TILE_STEP(K0, K1, V0, V1);
    const int nx = (it + 2 < NT) ? it + 2 : it;
    K0 = kv[nx * 256]; K1 = kv[nx * 256 + 64]; V0 = kv[nx * 256 + 128]; V1 = kv[nx * 256 + 192];
    TILE_STEP(nK0, nK1, nV0, nV1);
  }
  bool bad = false;
#pragma unroll
  for (int t = 0; t < T; ++t) {
    float l = hi ? O[t][8] : O[t][10];
    bad |= !(l < 1e30f);
#pragma unroll
    for (int r = 0; r < 10; ++r) bad |= !(fabsf(O[t][r]) < 1e36f);
  }
  if (__any(bad)) {
#pragma unroll
    for (int t = 0; t < T; ++t) {
      u32x4 x = __builtin_bit_cast(u32x4, Q1[t]); x[1] = 0u; bf16x8 q1 = __builtin_bit_cast(bf16x8, x);
      f32x16 acc = zero; float m = -3.0e38f;
      for (int it = 0; it < NT; ++it) {
        bf16x8 k0 = kv[it * 256], k1 = kv[it * 256 + 64], v0 = kv[it * 256 + 128], v1 = kv[it * 256 + 192];
        f32x16 s = MFMA32(k0, Q0[t], zero); s = MFMA32(k1, q1, s);
        float mn = fmaxf(m, xhalf_max(max16(s)));
        float a = __builtin_amdgcn_exp2f(m - mn); m = mn;
#pragma unroll
        for (int r = 0; r < 16; ++r) { acc[r] *= a; s[r] -= mn; }
        bf16x8 p0, p1; exp_pack(s, p0, p1);
        acc = MFMA32(v0, p0, acc); acc = MFMA32(v1, p1, acc);
      }
      O[t] = acc; mref[t] = m;
    }
  }
#pragma unroll
  for (int t = 0; t < T; ++t) {
    float* dst = red + ((rg * NSPLIT + sp) * T + t) * 12 * 64 + lane;
#pragma unroll
    for (int r = 0; r < 11; ++r) dst[r * 64] = O[t][r];
    dst[11 * 64] = mref[t];
  }
  __syncthreads();
  for (int t = sp; t < T; t += NSPLIT) {
    float mm = -3.0e38f;
    for (int w = 0; w < NSPLIT; ++w) mm = fmaxf(mm, red[((rg * NSPLIT + w) * T + t) * 12 * 64 + 11 * 64 + lane]);
    float acc[11];
    for (int r = 0; r < 11; ++r) acc[r] = 0.f;
    for (int w = 0; w < NSPLIT; ++w) {
      const float* src = red + ((rg * NSPLIT + w) * T + t) * 12 * 64 + lane;
      float f = __builtin_amdgcn_exp2f(src[11 * 64] - mm);
      for (int r = 0; r < 11; ++r) acc[r] += f * src[r * 64];
    }
    float l = hi ? acc[8] : acc[10]; float rl = 1.f / l;
    store_o(op, row0 + t, h, hi, acc, rl);
  }
}

constexpr int STG = 18560;
constexpr int REGB = 9344;
constexpr int RING = 4;
constexpr int LDS_GEMM = STG * RING;

template <int NST, bool NAT, class Src>
DI void gemm_core(LAS char* lds, const Src& src, const char* tptr, f32x16 (&acc)[9], int wid, int lane) {
  const bool weven = (wid & 1) == 0;
  const unsigned ldsbase = (unsigned)(size_t)lds;
  const unsigned ldsw = __builtin_amdgcn_readfirstlane(ldsbase + (wid >> 1) * REGB + (wid & 1) * 5120);
  const int G = lane >> 4, q = (lane & 15) >> 2, p = lane & 3, hi = lane >> 5;
  LAS char* trb = lds + (G & 1) * REGB + ((NAT ? 8 * hi : 4 * hi) + q) * 288 + 8 * p;
  const unsigned toff = lane * 16;
  u32x4 T[NST][2];
  bf16x8 A[NST * 18];
#define ISSUE(j, i) do { if ((j) < NST) { \
    if ((i) < 4) glds(src.voff((j), (i)), ldsw, ((j) % RING) * STG + (i) * 1024, src.wptr(j)); \
    else if ((i) == 4) { if (weven) glds(src.voff((j), 4), ldsw, ((j) % RING) * STG + 4096, src.wptr(j)); } \
    else if ((i) == 5) gload<0>(T[(j) < NST ? (j) : 0][0], toff, tptr + (j) * 2048); \
    else gload<1024>(T[(j) < NST ? (j) : 0][1], toff, tptr + (j) * 2048); } } while (0)
#define RD(g) do { if ((g) < NST * 18) { constexpr int j_ = 0; (void)j_; const int jj = (g) / 18, mm = (g) % 18, ss = mm / 9, bb = mm % 9; \
    A[(g) < NST * 18 ? (g) : 0] = trread(trb, (jj % RING) * STG + (16 * ss) * 288 + 32 * bb, (jj % RING) * STG + (16 * ss + (NAT ? 4 : 8)) * 288 + 32 * bb); } } while (0)
#pragma unroll
  for (int j = 0; j < 3; ++j)
#pragma unroll
    for (int i = 0; i < 7; ++i) ISSUE(j, i);
  asm volatile("s_waitcnt vmcnt(%2)" : "+v"(T[0][0]), "+v"(T[0][1]) : "i"(12) : "memory");
  __builtin_amdgcn_s_barrier();
  SCHED();
  RD(0); RD(1); RD(2);
  SCHED();
#pragma unroll
  for (int j = 0; j < NST; ++j) {
#pragma unroll
    for (int m = 0; m < 18; ++m) {
      const int g = j * 18 + m;
      if (m == 15 && j + 1 < NST) {
        if (j + 2 < NST) asm volatile("s_waitcnt vmcnt(%2)" : "+v"(T[j + 1 < NST ? j + 1 : 0][0]), "+v"(T[j + 1 < NST ? j + 1 : 0][1]) : "i"(6) : "memory");
        else asm volatile("s_waitcnt vmcnt(%2)" : "+v"(T[j + 1 < NST ? j + 1 : 0][0]), "+v"(T[j + 1 < NST ? j + 1 : 0][1]) : "i"(0) : "memory");
        __builtin_amdgcn_s_barrier();
        SCHED();
      }
      RD(g + 3);
      SCHED();
      acc[m % 9] = MFMA32(A[g], __builtin_bit_cast(bf16x8, T[j][m / 9]), acc[m % 9]);
      SCHED();
      if (m >= 15) ISSUE(j + 3, m - 15);
      else if (m < 4 && j >= 1) ISSUE(j + 2, m + 3);
      SCHED();
    }
  }
#undef ISSUE
#undef RD
}

DI void dma_rc(int wid, int lane, int i, int& row, int& c16) {
  const int ee = (wid & 1) * 5 + i;
  const int c = 64 * ee + lane;
  row = c / 18; c16 = c - row * 18;
}

struct SrcPlain {
  const char* base; unsigned stride; unsigned v[5];
  DI const char* wptr(int j) const { return base + (size_t)j * stride; }
  DI unsigned voff(int, int i) const { return v[i]; }
};
struct SrcOproj {
  const char* base; unsigned v[5], v2[5];
  DI const char* wptr(int j) const { return j < 16 ? base + (size_t)j * (36 * D * 2) : base + (size_t)(j - 16) * (288 * D * 2); }
  DI unsigned voff(int j, int i) const { return j < 16 ? v[i] : v2[i]; }
};

__global__ __launch_bounds__(256, 2) void k_gemm1(const u16* __restrict__ xnp, const u16* __restrict__ w1, const u16* __restrict__ w3, u16* __restrict__ hp, unsigned long long* __restrict__ tbuf) {
  __shared__ __attribute__((aligned(16))) char smem[LDS_GEMM];
  TDECL; RSTAMP(4); TSTAMP(0);
  const int wid = __builtin_amdgcn_readfirstlane(threadIdx.x >> 6), lane = threadIdx.x & 63;
  const int xcd = blockIdx.x & 7, idx = blockIdx.x >> 3;
  const int st = 4 * xcd + (idx & 3), tt = idx >> 2;
  const int tb = 4 * tt + wid;
  SrcPlain src;
  src.base = (const char*)((wid >> 1) ? w3 : w1) + st * 288;
  src.stride = 32 * F * 2;
#pragma unroll
  for (int i = 0; i < 5; ++i) { int r, c; dma_rc(wid, lane, i, r, c); src.v[i] = (unsigned)(r * F * 2 + c * 16); }
  f32x16 acc[9];
#pragma unroll
  for (int b = 0; b < 9; ++b) acc[b] = f32x16{};
  gemm_core<18, true>((LAS char*)smem, src, (const char*)xnp + (size_t)tb * 36 * 1024, acc, wid, lane);
  TSTAMP(1);
  char* hdst = (char*)hp + ((size_t)(tb * 288 + st * 9) * 64 + lane) * 16;
#pragma unroll
  for (int b = 0; b < 9; ++b) {
    float h[8];
#pragma unroll
    for (int j = 0; j < 8; ++j) h[j] = silu(acc[b][j]) * acc[b][8 + j];
    u32x4 o = {cvtpk(h[0], h[1]), cvtpk(h[2], h[3]), cvtpk(h[4], h[5]), cvtpk(h[6], h[7])};
    *(u32x4*)(hdst + b * 1024) = o;
  }
  TSTAMP(2); RSTAMP(5); TFLUSH();
}

DI void store_part(u16* __restrict__ P, int slot, int tb, int nh, int lane, const f32x16 (&acc)[9]) {
  char* dst = (char*)P + ((size_t)((slot * 64 + tb) * 18 + nh * 9) * 2 * 64 + lane) * 16;
#pragma unroll
  for (int b = 0; b < 9; ++b) {
    u32x4 o0 = {cvtpk(acc[b][0], acc[b][1]), cvtpk(acc[b][2], acc[b][3]), cvtpk(acc[b][4], acc[b][5]), cvtpk(acc[b][6], acc[b][7])};
    u32x4 o1 = {cvtpk(acc[b][8], acc[b][9]), cvtpk(acc[b][10], acc[b][11]), cvtpk(acc[b][12], acc[b][13]), cvtpk(acc[b][14], acc[b][15])};
    *(u32x4*)(dst + b * 2048) = o0;
    *(u32x4*)(dst + b * 2048 + 1024) = o1;
  }
}
__global__ __launch_bounds__(256, 2) void k_gemm2(const u16* __restrict__ hp, const u16* __restrict__ w2, const u16* __restrict__ op, const u16* __restrict__ wo, u16* __restrict__ P, unsigned long long* __restrict__ tbuf) {
  __shared__ __attribute__((aligned(16))) char smem[LDS_GEMM];
  TDECL; RSTAMP(4); TSTAMP(0);
  const int wid = __builtin_amdgcn_readfirstlane(threadIdx.x >> 6), lane = threadIdx.x & 63;
  const int xcd = blockIdx.x & 7, idx = blockIdx.x >> 3;
  const int nh = idx & 1, tt = idx >> 1;
  const int tb = 4 * tt + wid;
  const int col0 = nh * 288 + (wid >> 1) * 144;
  f32x16 acc[9];
#pragma unroll
  for (int b = 0; b < 9; ++b) acc[b] = f32x16{};
  if (xcd < 7) {
    SrcPlain src;
    const int f0 = 672 * xcd;
    src.base = (const char*)w2 + ((size_t)f0 * D + col0) * 2;
    src.stride = 32 * D * 2;
#pragma unroll
    for (int i = 0; i < 5; ++i) { int r, c; dma_rc(wid, lane, i, r, c); src.v[i] = (unsigned)(r * D * 2 + c * 16); }
    const char* tp = (const char*)hp + ((size_t)tb * 288 + (f0 >> 4)) * 1024;
    if (xcd < 6) gemm_core<21, false>((LAS char*)smem, src, tp, acc, wid, lane);
    else gemm_core<18, false>((LAS char*)smem, src, tp, acc, wid, lane);
  } else {
    SrcOproj src;
    src.base = (const char*)wo + col0 * 2;
#pragma unroll
    for (int i = 0; i < 5; ++i) {
      int r, c; dma_rc(wid, lane, i, r, c);
      src.v[i] = (unsigned)((18 * (r >> 4) + (r & 15)) * D * 2 + c * 16);
      src.v2[i] = (unsigned)((144 * (r >> 4) + 18 * ((r & 15) >> 1) + 16 + (r & 1)) * D * 2 + c * 16);
    }
    gemm_core<18, false>((LAS char*)smem, src, (const char*)op + (size_t)tb * 36 * 1024, acc, wid, lane);
  }
  TSTAMP(1);
  store_part(P, xcd, tb, nh, lane, acc);
  TSTAMP(2); RSTAMP(5); TFLUSH();
}

__global__ __launch_bounds__(576) void k_final(const u16* __restrict__ P, const u16* __restrict__ x, u16* __restrict__ out) {
  __shared__ float tile[32][148];
  const int tb = blockIdx.x >> 2, Q = blockIdx.x & 3, nh = Q >> 1, half = Q & 1;
  const int b = threadIdx.x >> 6, lane = threadIdx.x & 63, hi = lane >> 5, tok = lane & 31;
  const int r = threadIdx.x / 18, c8 = threadIdx.x - r * 18;
  const size_t goff = (size_t)(tb * 32 + r) * D + Q * 144 + c8 * 8;
  u32x4 xv = *(const u32x4*)(x + goff);
  u32x4 pv[8];
#pragma unroll
  for (int s = 0; s < 8; ++s) pv[s] = *(const u32x4*)((const char*)P + ((size_t)(((s * 64 + tb) * 18 + nh * 9 + b) * 2 + half) * 64 + lane) * 16);
  float v[8];
#pragma unroll
  for (int j = 0; j < 8; ++j) {
    float f = 0.f;
#pragma unroll
    for (int s = 0; s < 7; ++s) { unsigned w = pv[s][j >> 1]; f += __uint_as_float((j & 1) ? (w & 0xffff0000u) : (w << 16)); }
    unsigned w = pv[7][j >> 1]; float sv = __uint_as_float((j & 1) ? (w & 0xffff0000u) : (w << 16));
    v[j] = sv * silu(f);
  }
  *(f32x4*)&tile[tok][16 * b + 4 * hi] = f32x4{v[0], v[1], v[2], v[3]};
  *(f32x4*)&tile[tok][16 * b + 8 + 4 * hi] = f32x4{v[4], v[5], v[6], v[7]};
  __syncthreads();
  f32x4 a0 = *(const f32x4*)&tile[r][c8 * 8], a1 = *(const f32x4*)&tile[r][c8 * 8 + 4];
  float o[8];
#pragma unroll
  for (int j = 0; j < 8; ++j) { unsigned w = xv[j >> 1]; float xx = __uint_as_float((j & 1) ? (w & 0xffff0000u) : (w << 16)); o[j] = xx + (j < 4 ? a0[j] : a1[j - 4]); }
  u32x4 ov = {cvtpk(o[0], o[1]), cvtpk(o[2], o[3]), cvtpk(o[4], o[5]), cvtpk(o[6], o[7])};
  *(u32x4*)(out + goff) = ov;
}

#define AT_T 4
#define AT_NS 4
#define AT_RG 2

extern "C" void kernel_launch(void* const* d_in, const int* in_sizes, int n_in, void* d_out, int out_size, void* d_ws, size_t ws_size, hipStream_t stream) {
  const u16* x = (const u16*)d_in[0];
  const u16* wq = (const u16*)d_in[1];
  const u16* wk = (const u16*)d_in[2];
  const u16* wv = (const u16*)d_in[3];
  const u16* wo = (const u16*)d_in[4];
  const u16* w1 = (const u16*)d_in[6];
  const u16* w3 = (const u16*)d_in[7];
  const u16* w2 = (const u16*)d_in[8];
  u16* out = (u16*)d_out;
  u16* ws = (u16*)d_ws;
  u16* xn = ws;
  u16* q = xn + S * D;
  u16* k = q + S * D;
  u16* v = k + S * 32;
  u16* op = v + S * 32;
  u16* xnp = op + S * D;
  u16* hp = xnp + S * D;
  u16* P = hp + S * F;
  u16* kvf = P + 8 * S * D;
  unsigned long long* tb1 = (unsigned long long*)((char*)d_ws + (200u << 20));
  unsigned long long* tb2 = (unsigned long long*)((char*)d_ws + (201u << 20));
  k_pre<<<256, 256, 0, stream>>>(x, wq, wk, wv, xnp, q, kvf);
  k_attn<AT_T, AT_NS, AT_RG><<<S / (AT_T * AT_RG), 64 * AT_NS * AT_RG, 0, stream>>>(q, kvf, op);
  k_gemm1<<<512, 256, 0, stream>>>(xnp, w1, w3, hp, tb1);
  k_gemm2<<<256, 256, 0, stream>>>(hp, w2, op, wo, P, tb2);
  k_final<<<256, 576, 0, stream>>>(P, x, out);
}
```

```cpp
#include <hip/hip_runtime.h>
#include <hip/hip_bf16.h>
#include <stdint.h>

typedef unsigned short u16;
using bf16x8 = __attribute__((ext_vector_type(8))) short;
using v4s = __attribute__((ext_vector_type(4))) short;
using f32x4 = __attribute__((ext_vector_type(4))) float;
using f32x16 = __attribute__((ext_vector_type(16))) float;
using u32x4 = __attribute__((ext_vector_type(4))) unsigned;
using u32x2 = __attribute__((ext_vector_type(2))) unsigned;
#define DI __device__ __forceinline__
#define LAS __attribute__((address_space(3)))
#define MFMA32(a, b, c) __builtin_amdgcn_mfma_f32_32x32x16_bf16((a), (b), (c), 0, 0, 0)
#define SCHED() __builtin_amdgcn_sched_barrier(0)

constexpr int S = 2048, D = 576, H = 32, DH = 18, F = 4608;
constexpr float QSCALE = 0.23570226039551584f * 1.4426950408889634f;
constexpr float LOG2E = 1.4426950408889634f;

DI float bf2f(u16 v) { return __uint_as_float(((unsigned)v) << 16); }
DI u16 f2bf(float x) { unsigned u = __float_as_uint(x); u += 0x7fffu + ((u >> 16) & 1u); return (u16)(u >> 16); }
using f32x2 = __attribute__((ext_vector_type(2))) float;
using bf2 = __attribute__((ext_vector_type(2))) __bf16;
DI unsigned cvtpk(float lo, float hi) { f32x2 v = {lo, hi}; return __builtin_bit_cast(unsigned, __builtin_convertvector(v, bf2)); }
DI float silu(float a) { return a * __builtin_amdgcn_rcpf(1.f + __builtin_amdgcn_exp2f(-LOG2E * a)); }

#define TDECL
#define TSTAMP(i)
#define RSTAMP(i)
#define TFLUSH()

#include <cstdio>
#define TL_BEGIN() unsigned long long tl0_ = __builtin_amdgcn_s_memrealtime()
#define TL_END(kid) do { __syncthreads(); if (threadIdx.x == 0) { unsigned long long t1_ = __builtin_amdgcn_s_memrealtime(); tl[((kid) * 512 + blockIdx.x) * 2] = tl0_; tl[((kid) * 512 + blockIdx.x) * 2 + 1] = t1_; } } while (0)
__global__ void k_rep(const unsigned long long* tl) {
  __shared__ unsigned long long mn[5][64], mx[5][64], mn2[5][64], mx0[5][64];
  const int n[5] = {256, 256, 512, 256, 256};
  int t = threadIdx.x;
  for (int k = 0; k < 5; ++k) { unsigned long long a = ~0ull, b = 0, c = ~0ull, d = 0; for (int i = t; i < n[k]; i += 64) { unsigned long long s = tl[(k * 512 + i) * 2], e = tl[(k * 512 + i) * 2 + 1]; a = s < a ? s : a; b = e > b ? e : b; c = e < c ? e : c; d = s > d ? s : d; } mn[k][t] = a; mx[k][t] = b; mn2[k][t] = c; mx0[k][t] = d; }
  __syncthreads();
  if (t == 0) {
    unsigned long long A[5], B[5], C[5], Dd[5];
    for (int k = 0; k < 5; ++k) { A[k] = ~0ull; B[k] = 0; C[k] = ~0ull; Dd[k] = 0; for (int i = 0; i < 64; ++i) { A[k] = mn[k][i] < A[k] ? mn[k][i] : A[k]; B[k] = mx[k][i] > B[k] ? mx[k][i] : B[k]; C[k] = mn2[k][i] < C[k] ? mn2[k][i] : C[k]; Dd[k] = mx0[k][i] > Dd[k] ? mx0[k][i] : Dd[k]; } }
    int span = (int)(B[4] - A[0]);
    unsigned long long now = __builtin_amdgcn_s_memrealtime();
    bool pr = span > 7300 ? ((now & 3) == 0) : ((now & 127) == 0);
    if (pr) printf("[tl] span %d | pre s0 l%d fe%d e%d | attn s%d l%d fe%d e%d | g1 s%d l%d fe%d e%d | g2 s%d l%d fe%d e%d | fin s%d l%d fe%d e%d\n", span,
      (int)(Dd[0]-A[0]), (int)(C[0]-A[0]), (int)(B[0]-A[0]),
      (int)(A[1]-A[0]), (int)(Dd[1]-A[0]), (int)(C[1]-A[0]), (int)(B[1]-A[0]),
      (int)(A[2]-A[0]), (int)(Dd[2]-A[0]), (int)(C[2]-A[0]), (int)(B[2]-A[0]),
      (int)(A[3]-A[0]), (int)(Dd[3]-A[0]), (int)(C[3]-A[0]), (int)(B[3]-A[0]),
      (int)(A[4]-A[0]), (int)(Dd[4]-A[0]), (int)(C[4]-A[0]), (int)(B[4]-A[0]));
  }
}
#define MFMA16(a, b, c) __builtin_amdgcn_mfma_f32_16x16x32_bf16((a), (b), (c), 0, 0, 0)
typedef __attribute__((ext_vector_type(4))) short v4i16_t;
DI bf16x8 trread(LAS char* p, int imm0, int imm1) {
  v4i16_t a = __builtin_amdgcn_ds_read_tr16_b64_v4i16((LAS v4i16_t*)(p + imm0));
  v4i16_t b = __builtin_amdgcn_ds_read_tr16_b64_v4i16((LAS v4i16_t*)(p + imm1));
  return __builtin_shufflevector(a, b, 0, 1, 2, 3, 4, 5, 6, 7);
}
DI void glds(unsigned voff, unsigned ldsw, int imm, const char* g) {
  asm volatile("s_add_u32 m0, %1, %2\n\ts_nop 0\n\tglobal_load_lds_dwordx4 %0, %3" ::"v"(voff), "s"(ldsw), "i"(imm), "s"(g) : "memory", "scc");
}
template <int OFF>
DI void gload(u32x4& f, unsigned voff, const char* g) {
  asm volatile("global_load_dwordx4 %0, %1, %2 offset:%3" : "=v"(f) : "v"(voff), "s"(g), "i"(OFF) : "memory");
}
using u32x4u = __attribute__((ext_vector_type(4), aligned(4))) unsigned;
constexpr int PRE_LDS = 576 * 160;
template <bool LAST>
DI void pre_body(LAS char* lds, const u16* __restrict__ x, const u16* __restrict__ wq, const u16* __restrict__ wk, const u16* __restrict__ wv,
                 u16* __restrict__ xnp, u16* __restrict__ q, u16* __restrict__ kvf, int tg, int cg, int wid, int lane) {
  const int t16 = lane & 15, g = lane >> 4;
  const int row = tg * 64 + wid * 16 + t16;
  u32x4 X[18];
  {
    const unsigned xoff = (unsigned)(row * (D * 2) + g * 16);
    const char* xb = (const char*)x;
#define XL(kk) gload<(kk) * 64>(X[kk], xoff, xb)
    XL(0); XL(1); XL(2); XL(3); XL(4); XL(5); XL(6); XL(7); XL(8); XL(9); XL(10); XL(11); XL(12); XL(13); XL(14); XL(15); XL(16); XL(17);
#undef XL
  }
  if (!LAST) {
    const unsigned ldsw = __builtin_amdgcn_readfirstlane((unsigned)(size_t)lds + wid * 1024);
    unsigned vo[5];
#pragma unroll
    for (int i = 0; i < 5; ++i) {
      const int c = 256 * i + 64 * wid + lane, r = c / 10, c16 = c - r * 10;
      const int k = (r & ~31) + 8 * ((r >> 2) & 3) + 4 * ((r >> 4) & 1) + (r & 3);
      vo[i] = (unsigned)(k * (D * 2) + c16 * 16);
    }
    const char* wb = (const char*)wq + cg * 160;
#pragma unroll
    for (int i = 0; i < 23; ++i) {
      if (i < 22) glds(vo[i % 5], ldsw, i * 4096, wb + (i / 5) * (128 * D * 2));
      else if (wid < 2) glds(vo[i % 5], ldsw, i * 4096, wb + (i / 5) * (128 * D * 2));
    }
  } else {
#pragma unroll
    for (int it = 0; it < 3; ++it) {
      const int k = it * 256 + wid * 64 + lane;
      if (k < D) {
        const u32x4 q0 = *(const u32x4*)(wq + (size_t)k * D + 560), q1 = *(const u32x4*)(wq + (size_t)k * D + 568);
        const u32x4 k0 = *(const u32x4u*)(wk + k * DH), k1 = *(const u32x4u*)(wk + k * DH + 8);
        const u32x4 v0 = *(const u32x4u*)(wv + k * DH), v1 = *(const u32x4u*)(wv + k * DH + 8);
        const unsigned k2 = *(const unsigned*)(wk + k * DH + 16), v2 = *(const unsigned*)(wv + k * DH + 16);
        const int r = (k & ~31) + 16 * ((k >> 2) & 1) + 4 * ((k >> 3) & 3) + (k & 3);
        LAS char* d = lds + r * 160;
        *(LAS u32x4*)(d) = q0; *(LAS u32x4*)(d + 16) = q1; *(LAS u32x4*)(d + 32) = k0; *(LAS u32x4*)(d + 48) = k1;
        *(LAS u32x4*)(d + 64) = v0; *(LAS u32x4*)(d + 80) = v1; *(LAS u32x2*)(d + 96) = u32x2{v2, k2};
      }
    }
  }
  if (!LAST) {
    asm volatile("s_waitcnt vmcnt(22)" : "+v"(X[0]), "+v"(X[1]), "+v"(X[2]), "+v"(X[3]), "+v"(X[4]), "+v"(X[5]), "+v"(X[6]), "+v"(X[7]), "+v"(X[8]) :: "memory");
    asm volatile("" : "+v"(X[9]), "+v"(X[10]), "+v"(X[11]), "+v"(X[12]), "+v"(X[13]), "+v"(X[14]), "+v"(X[15]), "+v"(X[16]), "+v"(X[17]) :: "memory");
  } else {
    asm volatile("s_waitcnt vmcnt(0)" : "+v"(X[0]), "+v"(X[1]), "+v"(X[2]), "+v"(X[3]), "+v"(X[4]), "+v"(X[5]), "+v"(X[6]), "+v"(X[7]), "+v"(X[8]) :: "memory");
    asm volatile("" : "+v"(X[9]), "+v"(X[10]), "+v"(X[11]), "+v"(X[12]), "+v"(X[13]), "+v"(X[14]), "+v"(X[15]), "+v"(X[16]), "+v"(X[17]) :: "memory");
  }
  const u32x4 onesu = {0x3F803F80u, 0x3F803F80u, 0x3F803F80u, 0x3F803F80u};
  const bf16x8 ones = __builtin_bit_cast(bf16x8, onesu);
  f32x4 s4 = {}, g4 = {};
#pragma unroll
  for (int kk = 0; kk < 18; ++kk) {
    const bf16x8 xf = __builtin_bit_cast(bf16x8, X[kk]);
    s4 = MFMA16(ones, xf, s4);
    g4 = MFMA16(xf, xf, g4);
  }
  const int l3 = lane & 3;
  const float dsel = l3 == 0 ? g4[0] : (l3 == 1 ? g4[1] : (l3 == 2 ? g4[2] : g4[3]));
  const float ss = __shfl(dsel, t16 + 16 * (t16 >> 2));
  const float mean = s4[0] * (1.f / D);
  const float var = fmaxf(ss * (1.f / D) - mean * mean, 0.f);
  const float rstd = __builtin_amdgcn_rsqf(var + 1e-5f);
  const float nb = -mean * rstd;
#pragma unroll
  for (int kk = 0; kk < 18; ++kk) {
#pragma unroll
    for (int i = 0; i < 4; ++i) {
      const unsigned w = X[kk][i];
      X[kk][i] = cvtpk(fmaf(__uint_as_float(w << 16), rstd, nb), fmaf(__uint_as_float(w & 0xffff0000u), rstd, nb));
    }
  }
  asm volatile("" : "+v"(X[0]), "+v"(X[1]), "+v"(X[2]), "+v"(X[3]), "+v"(X[4]), "+v"(X[5]), "+v"(X[6]), "+v"(X[7]), "+v"(X[8]) :: "memory");
  asm volatile("" : "+v"(X[9]), "+v"(X[10]), "+v"(X[11]), "+v"(X[12]), "+v"(X[13]), "+v"(X[14]), "+v"(X[15]), "+v"(X[16]), "+v"(X[17]) :: "memory");
  f32x4 acc[5];
#pragma unroll
  for (int b = 0; b < 5; ++b) acc[b] = f32x4{};
  LAS char* trb = lds + (4 * g + (t16 >> 2)) * 160 + 8 * (lane & 3);
#pragma unroll
  for (int half = 0; half < 2; ++half) {
    if (!LAST) { if (half == 0) asm volatile("s_waitcnt vmcnt(11)" ::: "memory"); else asm volatile("s_waitcnt vmcnt(0)" ::: "memory"); }
    if (!LAST || half == 0) { asm volatile("s_waitcnt lgkmcnt(0)" ::: "memory"); __builtin_amdgcn_s_barrier(); }
    SCHED();
#pragma unroll
    for (int k9 = 0; k9 < 9; ++k9) {
      const int kk = half * 9 + k9;
      const bf16x8 xf = __builtin_bit_cast(bf16x8, X[kk]);
      if (!LAST) {
#pragma unroll
        for (int b = 0; b < 5; ++b) acc[b] = MFMA16(trread(trb, kk * 5120 + 32 * b, kk * 5120 + 2560 + 32 * b), xf, acc[b]);
      } else {
        acc[0] = MFMA16(trread(trb, kk * 5120, kk * 5120 + 2560), xf, acc[0]);
        acc[1] = MFMA16(trread(trb, kk * 5120 + 32, kk * 5120 + 2560 + 32), xf, acc[1]);
        acc[2] = MFMA16(xf, trread(trb, kk * 5120 + 64, kk * 5120 + 2560 + 64), acc[2]);
        const bf16x8 w3 = trread(trb, kk * 5120 + 96, kk * 5120 + 2560 + 96);
        acc[3] = MFMA16(xf, w3, acc[3]);
        acc[4] = MFMA16(w3, xf, acc[4]);
      }
    }
  }
  {
    const int tb = tg * 2 + (wid >> 1);
    char* xd = (char*)xnp + ((size_t)(tb * 36 + (g >> 1)) * 64 + (g & 1) * 32 + 16 * (wid & 1) + t16) * 16;
#pragma unroll
    for (int kk = 0; kk < 18; ++kk) if ((kk & 7) == cg) *(u32x4*)(xd + kk * 2048) = X[kk];
  }
  if (!LAST) {
    u16* qd = q + (size_t)row * D + cg * 80 + 4 * g;
#pragma unroll
    for (int b = 0; b < 5; ++b) *(u32x2*)(qd + 16 * b) = u32x2{cvtpk(acc[b][0] * QSCALE, acc[b][1] * QSCALE), cvtpk(acc[b][2] * QSCALE, acc[b][3] * QSCALE)};
  } else {
    *(u32x2*)(q + (size_t)row * D + 560 + 4 * g) = u32x2{cvtpk(acc[0][0] * QSCALE, acc[0][1] * QSCALE), cvtpk(acc[0][2] * QSCALE, acc[0][3] * QSCALE)};
    const int tile = tg * 2 + (wid >> 1), s = wid & 1, key = 16 * s + t16;
    char* kb = (char*)kvf + (size_t)tile * 4096;
    *(u32x2*)(kb + ((g >> 1) * 32 + key) * 16 + (g & 1) * 8) = u32x2{cvtpk(acc[1][0], acc[1][1]), cvtpk(acc[1][2], acc[1][3])};
    char* vb = kb + (2 + s) * 1024 + (g & 1) * 512 + (g >> 1) * 8;
    *(u32x2*)(vb + t16 * 16) = u32x2{cvtpk(acc[2][0], acc[2][1]), cvtpk(acc[2][2], acc[2][3])};
    const unsigned cst = (t16 == 2 || t16 == 4) ? 0x3F803F80u : 0u;
    u32x2 e = t16 < 2 ? u32x2{cvtpk(acc[3][0], acc[3][1]), cvtpk(acc[3][2], acc[3][3])} : u32x2{cst, cst};
    *(u32x2*)(vb + (16 + t16) * 16) = e;
    if (g < 2) {
      u32x4 k1 = g == 0 ? u32x4{cvtpk(acc[4][2], acc[4][3]), 0x00003F80u, 0u, 0u} : u32x4{0u, 0u, 0u, 0u};
      *(u32x4*)(kb + 1024 + (g * 32 + key) * 16) = k1;
    }
  }
}
__global__ __launch_bounds__(256) void k_pre(const u16* __restrict__ x, const u16* __restrict__ wq, const u16* __restrict__ wk, const u16* __restrict__ wv,
                                             u16* __restrict__ xnp, u16* __restrict__ q, u16* __restrict__ kvf, unsigned long long* tl) {
  TL_BEGIN();
  __shared__ __attribute__((aligned(16))) char smem[PRE_LDS];
  const int wid = __builtin_amdgcn_readfirstlane(threadIdx.x >> 6), lane = threadIdx.x & 63;
  const int xcd = blockIdx.x & 7, idx = blockIdx.x >> 3;
  const int tg = xcd * 4 + (idx >> 3), cg = idx & 7;
  if (cg < 7) pre_body<false>((LAS char*)smem, x, wq, wk, wv, xnp, q, kvf, tg, cg, wid, lane);
  else pre_body<true>((LAS char*)smem, x, wq, wk, wv, xnp, q, kvf, tg, cg, wid, lane);
  TL_END(0);
}

constexpr float MSHIFT = 80.f;
DI float xhalf_max(float v) { auto rr = __builtin_amdgcn_permlane32_swap(__float_as_uint(v), __float_as_uint(v), false, false); return fmaxf(__uint_as_float(rr[0]), __uint_as_float(rr[1])); }
DI float max16(const f32x16& s) {
  float a = fmaxf(fmaxf(s[0], s[1]), s[2]), b = fmaxf(fmaxf(s[3], s[4]), s[5]);
  a = fmaxf(fmaxf(a, s[6]), s[7]); b = fmaxf(fmaxf(b, s[8]), s[9]);
  a = fmaxf(fmaxf(a, s[10]), s[11]); b = fmaxf(fmaxf(b, s[12]), s[13]);
  a = fmaxf(fmaxf(a, s[14]), s[15]); return fmaxf(a, b);
}
DI void exp_pack(const f32x16& s, bf16x8& p0, bf16x8& p1) {
  u32x4 a, b;
#pragma unroll
  for (int i = 0; i < 4; ++i) a[i] = cvtpk(__builtin_amdgcn_exp2f(s[2 * i]), __builtin_amdgcn_exp2f(s[2 * i + 1]));
#pragma unroll
  for (int i = 0; i < 4; ++i) b[i] = cvtpk(__builtin_amdgcn_exp2f(s[8 + 2 * i]), __builtin_amdgcn_exp2f(s[8 + 2 * i + 1]));
  p0 = __builtin_bit_cast(bf16x8, a); p1 = __builtin_bit_cast(bf16x8, b);
}
DI void store_o(u16* __restrict__ op, int row, int h, int hi, const float* acc  , float rl) {
  const int tb = row >> 5, tok = row & 31;
  u32x4 a = {cvtpk(acc[0] * rl, acc[1] * rl), cvtpk(acc[2] * rl, acc[3] * rl), cvtpk(acc[4] * rl, acc[5] * rl), cvtpk(acc[6] * rl, acc[7] * rl)};
  *(u32x4*)(op + ((size_t)(tb * 36 + h) * 64 + hi * 32 + tok) * 8) = a;
  if (!hi) {
    const int hl = h & 7;
    *(unsigned*)(op + ((size_t)(tb * 36 + 32 + (h >> 3)) * 64 + ((hl >> 1) & 1) * 32 + tok) * 8 + (2 * (hl >> 2) + (hl & 1)) * 2) = cvtpk(acc[8] * rl, acc[9] * rl);
  }
}
template <int T, int NSPLIT, int RG>
__global__ __launch_bounds__(64 * NSPLIT * RG) void k_attn(const u16* __restrict__ q, const u16* __restrict__ kvf, u16* __restrict__ op, unsigned long long* tl) {
  TL_BEGIN();
  constexpr int NT = 64 / NSPLIT;
  __shared__ float red[RG * NSPLIT * T * 12 * 64];
  const int wid = threadIdx.x >> 6, lane = threadIdx.x & 63, hi = lane >> 5, h = lane & 31;
  const int rg = wid / NSPLIT, sp = wid % NSPLIT;
  const int row0 = (blockIdx.x * RG + rg) * T;
  bf16x8 Q0[T], Q1[T];
#pragma unroll
  for (int t = 0; t < T; ++t) {
    const u16* qp = q + (size_t)(row0 + t) * D + h * DH;
    Q0[t] = *(const bf16x8*)(qp + 8 * hi);
    unsigned w = *(const unsigned*)(qp + 16);
    u32x4 x = {hi ? 0u : w, 0u, 0u, 0u};
    Q1[t] = __builtin_bit_cast(bf16x8, x);
  }
  const bf16x8* kv = (const bf16x8*)kvf + (size_t)sp * NT * 256 + lane;
  bf16x8 K0 = kv[0], K1 = kv[64], V0 = kv[128], V1 = kv[192];
  float mref[T];
  const f32x16 zero = {};
#pragma unroll
  for (int t = 0; t < T; ++t) {
    f32x16 s = MFMA32(K0, Q0[t], zero); s = MFMA32(K1, Q1[t], s);
    float m = xhalf_max(max16(s)) + MSHIFT;
    unsigned mb = (unsigned)f2bf(-m);
    mref[t] = -bf2f((u16)mb);
    u32x4 x = __builtin_bit_cast(u32x4, Q1[t]); x[1] = hi ? 0u : mb; Q1[t] = __builtin_bit_cast(bf16x8, x);
  }
  f32x16 O[T];
#pragma unroll
  for (int t = 0; t < T; ++t) O[t] = zero;
#define TILE_STEP(K0_, K1_, V0_, V1_)                                   \
  _Pragma("unroll") for (int t = 0; t < T; ++t) {                          \
    f32x16 s = MFMA32(K0_, Q0[t], zero); s = MFMA32(K1_, Q1[t], s);        \
    bf16x8 p0, p1; exp_pack(s, p0, p1);                                    \
    O[t] = MFMA32(V0_, p0, O[t]); O[t] = MFMA32(V1_, p1, O[t]);            \
  }
  for (int it = 0; it < NT; it += 2) {
    bf16x8 nK0 = kv[(it + 1) * 256], nK1 = kv[(it + 1) * 256 + 64], nV0 = kv[(it + 1) * 256 + 128], nV1 = kv[(it + 1) * 256 + 192];
    TILE_STEP(K0, K1, V0, V1);
    const int nx = (it + 2 < NT) ? it + 2 : it;
    K0 = kv[nx * 256]; K1 = kv[nx * 256 + 64]; V0 = kv[nx * 256 + 128]; V1 = kv[nx * 256 + 192];
    TILE_STEP(nK0, nK1, nV0, nV1);
  }
  bool bad = false;
#pragma unroll
  for (int t = 0; t < T; ++t) {
    float l = hi ? O[t][8] : O[t][10];
    bad |= !(l < 1e30f);
#pragma unroll
    for (int r = 0; r < 10; ++r) bad |= !(fabsf(O[t][r]) < 1e36f);
  }
  if (__any(bad)) {
#pragma unroll
    for (int t = 0; t < T; ++t) {
      u32x4 x = __builtin_bit_cast(u32x4, Q1[t]); x[1] = 0u; bf16x8 q1 = __builtin_bit_cast(bf16x8, x);
      f32x16 acc = zero; float m = -3.0e38f;
      for (int it = 0; it < NT; ++it) {
        bf16x8 k0 = kv[it * 256], k1 = kv[it * 256 + 64], v0 = kv[it * 256 + 128], v1 = kv[it * 256 + 192];
        f32x16 s = MFMA32(k0, Q0[t], zero); s = MFMA32(k1, q1, s);
        float mn = fmaxf(m, xhalf_max(max16(s)));
        float a = __builtin_amdgcn_exp2f(m - mn); m = mn;
#pragma unroll
        for (int r = 0; r < 16; ++r) { acc[r] *= a; s[r] -= mn; }
        bf16x8 p0, p1; exp_pack(s, p0, p1);
        acc = MFMA32(v0, p0, acc); acc = MFMA32(v1, p1, acc);
      }
      O[t] = acc; mref[t] = m;
    }
  }
#pragma unroll
  for (int t = 0; t < T; ++t) {
    float* dst = red + ((rg * NSPLIT + sp) * T + t) * 12 * 64 + lane;
#pragma unroll
    for (int r = 0; r < 11; ++r) dst[r * 64] = O[t][r];
    dst[11 * 64] = mref[t];
  }
  __syncthreads();
  for (int t = sp; t < T; t += NSPLIT) {
    float mm = -3.0e38f;
    for (int w = 0; w < NSPLIT; ++w) mm = fmaxf(mm, red[((rg * NSPLIT + w) * T + t) * 12 * 64 + 11 * 64 + lane]);
    float acc[11];
    for (int r = 0; r < 11; ++r) acc[r] = 0.f;
    for (int w = 0; w < NSPLIT; ++w) {
      const float* src = red + ((rg * NSPLIT + w) * T + t) * 12 * 64 + lane;
      float f = __builtin_amdgcn_exp2f(src[11 * 64] - mm);
      for (int r = 0; r < 11; ++r) acc[r] += f * src[r * 64];
    }
    float l = hi ? acc[8] : acc[10]; float rl = 1.f / l;
    store_o(op, row0 + t, h, hi, acc, rl);
  }
  TL_END(1);
}

constexpr int STG = 18560;
constexpr int REGB = 9344;
constexpr int RING = 4;
constexpr int LDS_GEMM = STG * RING;

template <int NST, bool NAT, class Src>
DI void gemm_core(LAS char* lds, const Src& src, const char* tptr, f32x16 (&acc)[9], int wid, int lane) {
  const bool weven = (wid & 1) == 0;
  const unsigned ldsbase = (unsigned)(size_t)lds;
  const unsigned ldsw = __builtin_amdgcn_readfirstlane(ldsbase + (wid >> 1) * REGB + (wid & 1) * 5120);
  const int G = lane >> 4, q = (lane & 15) >> 2, p = lane & 3, hi = lane >> 5;
  LAS char* trb = lds + (G & 1) * REGB + ((NAT ? 8 * hi : 4 * hi) + q) * 288 + 8 * p;
  const unsigned toff = lane * 16;
  u32x4 T[NST][2];
  bf16x8 A[NST * 18];
#define ISSUE(j, i) do { if ((j) < NST) { \
    if ((i) < 4) glds(src.voff((j), (i)), ldsw, ((j) % RING) * STG + (i) * 1024, src.wptr(j)); \
    else if ((i) == 4) { if (weven) glds(src.voff((j), 4), ldsw, ((j) % RING) * STG + 4096, src.wptr(j)); } \
    else if ((i) == 5) gload<0>(T[(j) < NST ? (j) : 0][0], toff, tptr + (j) * 2048); \
    else gload<1024>(T[(j) < NST ? (j) : 0][1], toff, tptr + (j) * 2048); } } while (0)
#define RD(g) do { if ((g) < NST * 18) { constexpr int j_ = 0; (void)j_; const int jj = (g) / 18, mm = (g) % 18, ss = mm / 9, bb = mm % 9; \
    A[(g) < NST * 18 ? (g) : 0] = trread(trb, (jj % RING) * STG + (16 * ss) * 288 + 32 * bb, (jj % RING) * STG + (16 * ss + (NAT ? 4 : 8)) * 288 + 32 * bb); } } while (0)
#pragma unroll
  for (int j = 0; j < 3; ++j)
#pragma unroll
    for (int i = 0; i < 7; ++i) ISSUE(j, i);
  asm volatile("s_waitcnt vmcnt(%2)" : "+v"(T[0][0]), "+v"(T[0][1]) : "i"(12) : "memory");
  __builtin_amdgcn_s_barrier();
  SCHED();
  RD(0); RD(1); RD(2);
  SCHED();
#pragma unroll
  for (int j = 0; j < NST; ++j) {
#pragma unroll
    for (int m = 0; m < 18; ++m) {
      const int g = j * 18 + m;
      if (m == 15 && j + 1 < NST) {
        if (j + 2 < NST) asm volatile("s_waitcnt vmcnt(%2)" : "+v"(T[j + 1 < NST ? j + 1 : 0][0]), "+v"(T[j + 1 < NST ? j + 1 : 0][1]) : "i"(6) : "memory");
        else asm volatile("s_waitcnt vmcnt(%2)" : "+v"(T[j + 1 < NST ? j + 1 : 0][0]), "+v"(T[j + 1 < NST ? j + 1 : 0][1]) : "i"(0) : "memory");
        __builtin_amdgcn_s_barrier();
        SCHED();
      }
      RD(g + 3);
      SCHED();
      acc[m % 9] = MFMA32(A[g], __builtin_bit_cast(bf16x8, T[j][m / 9]), acc[m % 9]);
      SCHED();
      if (m >= 15) ISSUE(j + 3, m - 15);
      else if (m < 4 && j >= 1) ISSUE(j + 2, m + 3);
      SCHED();
    }
  }
#undef ISSUE
#undef RD
}

DI void dma_rc(int wid, int lane, int i, int& row, int& c16) {
  const int ee = (wid & 1) * 5 + i;
  const int c = 64 * ee + lane;
  row = c / 18; c16 = c - row * 18;
}

struct SrcPlain {
  const char* base; unsigned stride; unsigned v[5];
  DI const char* wptr(int j) const { return base + (size_t)j * stride; }
  DI unsigned voff(int, int i) const { return v[i]; }
};
struct SrcOproj {
  const char* base; unsigned v[5], v2[5];
  DI const char* wptr(int j) const { return j < 16 ? base + (size_t)j * (36 * D * 2) : base + (size_t)(j - 16) * (288 * D * 2); }
  DI unsigned voff(int j, int i) const { return j < 16 ? v[i] : v2[i]; }
};

__global__ __launch_bounds__(256, 2) void k_gemm1(const u16* __restrict__ xnp, const u16* __restrict__ w1, const u16* __restrict__ w3, u16* __restrict__ hp, unsigned long long* __restrict__ tl) {
  TL_BEGIN();
  __shared__ __attribute__((aligned(16))) char smem[LDS_GEMM];
  TDECL; RSTAMP(4); TSTAMP(0);
  const int wid = __builtin_amdgcn_readfirstlane(threadIdx.x >> 6), lane = threadIdx.x & 63;
  const int xcd = blockIdx.x & 7, idx = blockIdx.x >> 3;
  const int st = 4 * xcd + (idx & 3), tt = idx >> 2;
  const int tb = 4 * tt + wid;
  SrcPlain src;
  src.base = (const char*)((wid >> 1) ? w3 : w1) + st * 288;
  src.stride = 32 * F * 2;
#pragma unroll
  for (int i = 0; i < 5; ++i) { int r, c; dma_rc(wid, lane, i, r, c); src.v[i] = (unsigned)(r * F * 2 + c * 16); }
  f32x16 acc[9];
#pragma unroll
  for (int b = 0; b < 9; ++b) acc[b] = f32x16{};
  gemm_core<18, true>((LAS char*)smem, src, (const char*)xnp + (size_t)tb * 36 * 1024, acc, wid, lane);
  TSTAMP(1);
  char* hdst = (char*)hp + ((size_t)(tb * 288 + st * 9) * 64 + lane) * 16;
#pragma unroll
  for (int b = 0; b < 9; ++b) {
    float h[8];
#pragma unroll
    for (int j = 0; j < 8; ++j) h[j] = silu(acc[b][j]) * acc[b][8 + j];
    u32x4 o = {cvtpk(h[0], h[1]), cvtpk(h[2], h[3]), cvtpk(h[4], h[5]), cvtpk(h[6], h[7])};
    *(u32x4*)(hdst + b * 1024) = o;
  }
  TL_END(2);
}

DI void store_part(u16* __restrict__ P, int slot, int tb, int nh, int lane, const f32x16 (&acc)[9]) {
  char* dst = (char*)P + ((size_t)((slot * 64 + tb) * 18 + nh * 9) * 2 * 64 + lane) * 16;
#pragma unroll
  for (int b = 0; b < 9; ++b) {
    u32x4 o0 = {cvtpk(acc[b][0], acc[b][1]), cvtpk(acc[b][2], acc[b][3]), cvtpk(acc[b][4], acc[b][5]), cvtpk(acc[b][6], acc[b][7])};
    u32x4 o1 = {cvtpk(acc[b][8], acc[b][9]), cvtpk(acc[b][10], acc[b][11]), cvtpk(acc[b][12], acc[b][13]), cvtpk(acc[b][14], acc[b][15])};
    *(u32x4*)(dst + b * 2048) = o0;
    *(u32x4*)(dst + b * 2048 + 1024) = o1;
  }
}
__global__ __launch_bounds__(256, 2) void k_gemm2(const u16* __restrict__ hp, const u16* __restrict__ w2, const u16* __restrict__ op, const u16* __restrict__ wo, u16* __restrict__ P, unsigned long long* __restrict__ tl) {
  TL_BEGIN();
  __shared__ __attribute__((aligned(16))) char smem[LDS_GEMM];
  TDECL; RSTAMP(4); TSTAMP(0);
  const int wid = __builtin_amdgcn_readfirstlane(threadIdx.x >> 6), lane = threadIdx.x & 63;
  const int xcd = blockIdx.x & 7, idx = blockIdx.x >> 3;
  const int nh = idx & 1, tt = idx >> 1;
  const int tb = 4 * tt + wid;
  const int col0 = nh * 288 + (wid >> 1) * 144;
  f32x16 acc[9];
#pragma unroll
  for (int b = 0; b < 9; ++b) acc[b] = f32x16{};
  if (xcd < 7) {
    SrcPlain src;
    const int f0 = 672 * xcd;
    src.base = (const char*)w2 + ((size_t)f0 * D + col0) * 2;
    src.stride = 32 * D * 2;
#pragma unroll
    for (int i = 0; i < 5; ++i) { int r, c; dma_rc(wid, lane, i, r, c); src.v[i] = (unsigned)(r * D * 2 + c * 16); }
    const char* tp = (const char*)hp + ((size_t)tb * 288 + (f0 >> 4)) * 1024;
    if (xcd < 6) gemm_core<21, false>((LAS char*)smem, src, tp, acc, wid, lane);
    else gemm_core<18, false>((LAS char*)smem, src, tp, acc, wid, lane);
  } else {
    SrcOproj src;
    src.base = (const char*)wo + col0 * 2;
#pragma unroll
    for (int i = 0; i < 5; ++i) {
      int r, c; dma_rc(wid, lane, i, r, c);
      src.v[i] = (unsigned)((18 * (r >> 4) + (r & 15)) * D * 2 + c * 16);
      src.v2[i] = (unsigned)((144 * (r >> 4) + 18 * ((r & 15) >> 1) + 16 + (r & 1)) * D * 2 + c * 16);
    }
    gemm_core<18, false>((LAS char*)smem, src, (const char*)op + (size_t)tb * 36 * 1024, acc, wid, lane);
  }
  TSTAMP(1);
  store_part(P, xcd, tb, nh, lane, acc);
  TL_END(3);
}

__global__ __launch_bounds__(576) void k_final(const u16* __restrict__ P, const u16* __restrict__ x, u16* __restrict__ out, unsigned long long* tl) {
  TL_BEGIN();
  __shared__ float tile[32][148];
  const int tb = blockIdx.x >> 2, Q = blockIdx.x & 3, nh = Q >> 1, half = Q & 1;
  const int b = threadIdx.x >> 6, lane = threadIdx.x & 63, hi = lane >> 5, tok = lane & 31;
  const int r = threadIdx.x / 18, c8 = threadIdx.x - r * 18;
  const size_t goff = (size_t)(tb * 32 + r) * D + Q * 144 + c8 * 8;
  u32x4 xv = *(const u32x4*)(x + goff);
  u32x4 pv[8];
#pragma unroll
  for (int s = 0; s < 8; ++s) pv[s] = *(const u32x4*)((const char*)P + ((size_t)(((s * 64 + tb) * 18 + nh * 9 + b) * 2 + half) * 64 + lane) * 16);
  float v[8];
#pragma unroll
  for (int j = 0; j < 8; ++j) {
    float f = 0.f;
#pragma unroll
    for (int s = 0; s < 7; ++s) { unsigned w = pv[s][j >> 1]; f += __uint_as_float((j & 1) ? (w & 0xffff0000u) : (w << 16)); }
    unsigned w = pv[7][j >> 1]; float sv = __uint_as_float((j & 1) ? (w & 0xffff0000u) : (w << 16));
    v[j] = sv * silu(f);
  }
  *(f32x4*)&tile[tok][16 * b + 4 * hi] = f32x4{v[0], v[1], v[2], v[3]};
  *(f32x4*)&tile[tok][16 * b + 8 + 4 * hi] = f32x4{v[4], v[5], v[6], v[7]};
  __syncthreads();
  f32x4 a0 = *(const f32x4*)&tile[r][c8 * 8], a1 = *(const f32x4*)&tile[r][c8 * 8 + 4];
  float o[8];
#pragma unroll
  for (int j = 0; j < 8; ++j) { unsigned w = xv[j >> 1]; float xx = __uint_as_float((j & 1) ? (w & 0xffff0000u) : (w << 16)); o[j] = xx + (j < 4 ? a0[j] : a1[j - 4]); }
  u32x4 ov = {cvtpk(o[0], o[1]), cvtpk(o[2], o[3]), cvtpk(o[4], o[5]), cvtpk(o[6], o[7])};
  *(u32x4*)(out + goff) = ov;
  TL_END(4);
}

#define AT_T 4
#define AT_NS 4
#define AT_RG 2

extern "C" void kernel_launch(void* const* d_in, const int* in_sizes, int n_in, void* d_out, int out_size, void* d_ws, size_t ws_size, hipStream_t stream) {
  const u16* x = (const u16*)d_in[0];
  const u16* wq = (const u16*)d_in[1];
  const u16* wk = (const u16*)d_in[2];
  const u16* wv = (const u16*)d_in[3];
  const u16* wo = (const u16*)d_in[4];
  const u16* w1 = (const u16*)d_in[6];
  const u16* w3 = (const u16*)d_in[7];
  const u16* w2 = (const u16*)d_in[8];
  u16* out = (u16*)d_out;
  u16* ws = (u16*)d_ws;
  u16* xn = ws;
  u16* q = xn + S * D;
  u16* k = q + S * D;
  u16* v = k + S * 32;
  u16* op = v + S * 32;
  u16* xnp = op + S * D;
  u16* hp = xnp + S * D;
  u16* P = hp + S * F;
  u16* kvf = P + 8 * S * D;
  unsigned long long* tb1 = (unsigned long long*)((char*)d_ws + (200u << 20));
  unsigned long long* tb2 = (unsigned long long*)((char*)d_ws + (201u << 20));
  k_pre<<<256, 256, 0, stream>>>(x, wq, wk, wv, xnp, q, kvf, tb1);
  k_attn<AT_T, AT_NS, AT_RG><<<S / (AT_T * AT_RG), 64 * AT_NS * AT_RG, 0, stream>>>(q, kvf, op, tb1);
  k_gemm1<<<512, 256, 0, stream>>>(xnp, w1, w3, hp, tb1);
  k_gemm2<<<256, 256, 0, stream>>>(hp, w2, op, wo, P, tb1);
  k_final<<<256, 576, 0, stream>>>(P, x, out, tb1);
  k_rep<<<1, 64, 0, stream>>>(tb1);
}
```

```cpp
#include <hip/hip_runtime.h>
#include <hip/hip_bf16.h>
#include <stdint.h>

#define PF 3
typedef unsigned short u16;
using bf16x8 = __attribute__((ext_vector_type(8))) short;
using v4s = __attribute__((ext_vector_type(4))) short;
using f32x4 = __attribute__((ext_vector_type(4))) float;
using f32x16 = __attribute__((ext_vector_type(16))) float;
using u32x4 = __attribute__((ext_vector_type(4))) unsigned;
using u32x2 = __attribute__((ext_vector_type(2))) unsigned;
#define DI __device__ __forceinline__
#define LAS __attribute__((address_space(3)))
#define MFMA32(a, b, c) __builtin_amdgcn_mfma_f32_32x32x16_bf16((a), (b), (c), 0, 0, 0)
#define SCHED() __builtin_amdgcn_sched_barrier(0)

constexpr int S = 2048, D = 576, H = 32, DH = 18, F = 4608;
constexpr float QSCALE = 0.23570226039551584f * 1.4426950408889634f;
constexpr float LOG2E = 1.4426950408889634f;

DI float bf2f(u16 v) { return __uint_as_float(((unsigned)v) << 16); }
DI u16 f2bf(float x) { unsigned u = __float_as_uint(x); u += 0x7fffu + ((u >> 16) & 1u); return (u16)(u >> 16); }
using f32x2 = __attribute__((ext_vector_type(2))) float;
using bf2 = __attribute__((ext_vector_type(2))) __bf16;
DI unsigned cvtpk(float lo, float hi) { f32x2 v = {lo, hi}; return __builtin_bit_cast(unsigned, __builtin_convertvector(v, bf2)); }
DI float silu(float a) { return a * __builtin_amdgcn_rcpf(1.f + __builtin_amdgcn_exp2f(-LOG2E * a)); }

#define TDECL
#define TSTAMP(i)
#define RSTAMP(i)
#define TFLUSH()

#define MFMA16(a, b, c) __builtin_amdgcn_mfma_f32_16x16x32_bf16((a), (b), (c), 0, 0, 0)
typedef __attribute__((ext_vector_type(4))) short v4i16_t;
DI bf16x8 trread(LAS char* p, int imm0, int imm1) {
  v4i16_t a = __builtin_amdgcn_ds_read_tr16_b64_v4i16((LAS v4i16_t*)(p + imm0));
  v4i16_t b = __builtin_amdgcn_ds_read_tr16_b64_v4i16((LAS v4i16_t*)(p + imm1));
  return __builtin_shufflevector(a, b, 0, 1, 2, 3, 4, 5, 6, 7);
}
DI void glds(unsigned voff, unsigned ldsw, int imm, const char* g) {
  asm volatile("s_add_u32 m0, %1, %2\n\ts_nop 0\n\tglobal_load_lds_dwordx4 %0, %3" ::"v"(voff), "s"(ldsw), "i"(imm), "s"(g) : "memory", "scc");
}
template <int OFF>
DI void gload(u32x4& f, unsigned voff, const char* g) {
  asm volatile("global_load_dwordx4 %0, %1, %2 offset:%3" : "=v"(f) : "v"(voff), "s"(g), "i"(OFF) : "memory");
}
using u32x4u = __attribute__((ext_vector_type(4), aligned(4))) unsigned;
constexpr int PRE_LDS = 576 * 160;
template <bool LAST>
DI void pre_body(LAS char* lds, const u16* __restrict__ x, const u16* __restrict__ wq, const u16* __restrict__ wk, const u16* __restrict__ wv,
                 u16* __restrict__ xnp, u16* __restrict__ q, u16* __restrict__ kvf, int tg, int cg, int wid, int lane) {
  const int t16 = lane & 15, g = lane >> 4;
  const int row = tg * 64 + wid * 16 + t16;
  u32x4 X[18];
  {
    const unsigned xoff = (unsigned)(row * (D * 2) + g * 16);
    const char* xb = (const char*)x;
#define XL(kk) gload<(kk) * 64>(X[kk], xoff, xb)
    XL(0); XL(1); XL(2); XL(3); XL(4); XL(5); XL(6); XL(7); XL(8); XL(9); XL(10); XL(11); XL(12); XL(13); XL(14); XL(15); XL(16); XL(17);
#undef XL
  }
  if (!LAST) {
    const unsigned ldsw = __builtin_amdgcn_readfirstlane((unsigned)(size_t)lds + wid * 1024);
    unsigned vo[5];
#pragma unroll
    for (int i = 0; i < 5; ++i) {
      const int c = 256 * i + 64 * wid + lane, r = c / 10, c16 = c - r * 10;
      const int k = (r & ~31) + 8 * ((r >> 2) & 3) + 4 * ((r >> 4) & 1) + (r & 3);
      vo[i] = (unsigned)(k * (D * 2) + c16 * 16);
    }
    const char* wb = (const char*)wq + cg * 160;
#pragma unroll
    for (int i = 0; i < 23; ++i) {
      if (i < 22) glds(vo[i % 5], ldsw, i * 4096, wb + (i / 5) * (128 * D * 2));
      else if (wid < 2) glds(vo[i % 5], ldsw, i * 4096, wb + (i / 5) * (128 * D * 2));
    }
  } else {
#pragma unroll
    for (int it = 0; it < 3; ++it) {
      const int k = it * 256 + wid * 64 + lane;
      if (k < D) {
        const u32x4 q0 = *(const u32x4*)(wq + (size_t)k * D + 560), q1 = *(const u32x4*)(wq + (size_t)k * D + 568);
        const u32x4 k0 = *(const u32x4u*)(wk + k * DH), k1 = *(const u32x4u*)(wk + k * DH + 8);
        const u32x4 v0 = *(const u32x4u*)(wv + k * DH), v1 = *(const u32x4u*)(wv + k * DH + 8);
        const unsigned k2 = *(const unsigned*)(wk + k * DH + 16), v2 = *(const unsigned*)(wv + k * DH + 16);
        const int r = (k & ~31) + 16 * ((k >> 2) & 1) + 4 * ((k >> 3) & 3) + (k & 3);
        LAS char* d = lds + r * 160;
        *(LAS u32x4*)(d) = q0; *(LAS u32x4*)(d + 16) = q1; *(LAS u32x4*)(d + 32) = k0; *(LAS u32x4*)(d + 48) = k1;
        *(LAS u32x4*)(d + 64) = v0; *(LAS u32x4*)(d + 80) = v1; *(LAS u32x2*)(d + 96) = u32x2{v2, k2};
      }
    }
  }
  if (!LAST) {
    asm volatile("s_waitcnt vmcnt(22)" : "+v"(X[0]), "+v"(X[1]), "+v"(X[2]), "+v"(X[3]), "+v"(X[4]), "+v"(X[5]), "+v"(X[6]), "+v"(X[7]), "+v"(X[8]) :: "memory");
    asm volatile("" : "+v"(X[9]), "+v"(X[10]), "+v"(X[11]), "+v"(X[12]), "+v"(X[13]), "+v"(X[14]), "+v"(X[15]), "+v"(X[16]), "+v"(X[17]) :: "memory");
  } else {
    asm volatile("s_waitcnt vmcnt(0)" : "+v"(X[0]), "+v"(X[1]), "+v"(X[2]), "+v"(X[3]), "+v"(X[4]), "+v"(X[5]), "+v"(X[6]), "+v"(X[7]), "+v"(X[8]) :: "memory");
    asm volatile("" : "+v"(X[9]), "+v"(X[10]), "+v"(X[11]), "+v"(X[12]), "+v"(X[13]), "+v"(X[14]), "+v"(X[15]), "+v"(X[16]), "+v"(X[17]) :: "memory");
  }
  const u32x4 onesu = {0x3F803F80u, 0x3F803F80u, 0x3F803F80u, 0x3F803F80u};
  const bf16x8 ones = __builtin_bit_cast(bf16x8, onesu);
  f32x4 s4 = {}, g4 = {};
#pragma unroll
  for (int kk = 0; kk < 18; ++kk) {
    const bf16x8 xf = __builtin_bit_cast(bf16x8, X[kk]);
    s4 = MFMA16(ones, xf, s4);
    g4 = MFMA16(xf, xf, g4);
  }
  const int l3 = lane & 3;
  const float dsel = l3 == 0 ? g4[0] : (l3 == 1 ? g4[1] : (l3 == 2 ? g4[2] : g4[3]));
  const float ss = __shfl(dsel, t16 + 16 * (t16 >> 2));
  const float mean = s4[0] * (1.f / D);
  const float var = fmaxf(ss * (1.f / D) - mean * mean, 0.f);
  const float rstd = __builtin_amdgcn_rsqf(var + 1e-5f);
  const float nb = -mean * rstd;
#pragma unroll
  for (int kk = 0; kk < 18; ++kk) {
#pragma unroll
    for (int i = 0; i < 4; ++i) {
      const unsigned w = X[kk][i];
      X[kk][i] = cvtpk(fmaf(__uint_as_float(w << 16), rstd, nb), fmaf(__uint_as_float(w & 0xffff0000u), rstd, nb));
    }
  }
  asm volatile("" : "+v"(X[0]), "+v"(X[1]), "+v"(X[2]), "+v"(X[3]), "+v"(X[4]), "+v"(X[5]), "+v"(X[6]), "+v"(X[7]), "+v"(X[8]) :: "memory");
  asm volatile("" : "+v"(X[9]), "+v"(X[10]), "+v"(X[11]), "+v"(X[12]), "+v"(X[13]), "+v"(X[14]), "+v"(X[15]), "+v"(X[16]), "+v"(X[17]) :: "memory");
  f32x4 acc[5];
#pragma unroll
  for (int b = 0; b < 5; ++b) acc[b] = f32x4{};
  LAS char* trb = lds + (4 * g + (t16 >> 2)) * 160 + 8 * (lane & 3);
#pragma unroll
  for (int half = 0; half < 2; ++half) {
    if (!LAST) { if (half == 0) asm volatile("s_waitcnt vmcnt(11)" ::: "memory"); else asm volatile("s_waitcnt vmcnt(0)" ::: "memory"); }
    if (!LAST || half == 0) { asm volatile("s_waitcnt lgkmcnt(0)" ::: "memory"); __builtin_amdgcn_s_barrier(); }
    SCHED();
#pragma unroll
    for (int k9 = 0; k9 < 9; ++k9) {
      const int kk = half * 9 + k9;
      const bf16x8 xf = __builtin_bit_cast(bf16x8, X[kk]);
      if (!LAST) {
#pragma unroll
        for (int b = 0; b < 5; ++b) acc[b] = MFMA16(trread(trb, kk * 5120 + 32 * b, kk * 5120 + 2560 + 32 * b), xf, acc[b]);
      } else {
        acc[0] = MFMA16(trread(trb, kk * 5120, kk * 5120 + 2560), xf, acc[0]);
        acc[1] = MFMA16(trread(trb, kk * 5120 + 32, kk * 5120 + 2560 + 32), xf, acc[1]);
        acc[2] = MFMA16(xf, trread(trb, kk * 5120 + 64, kk * 5120 + 2560 + 64), acc[2]);
        const bf16x8 w3 = trread(trb, kk * 5120 + 96, kk * 5120 + 2560 + 96);
        acc[3] = MFMA16(xf, w3, acc[3]);
        acc[4] = MFMA16(w3, xf, acc[4]);
      }
    }
  }
  {
    const int tb = tg * 2 + (wid >> 1);
    char* xd = (char*)xnp + ((size_t)(tb * 36 + (g >> 1)) * 64 + (g & 1) * 32 + 16 * (wid & 1) + t16) * 16;
#pragma unroll
    for (int kk = 0; kk < 18; ++kk) if ((kk & 7) == cg) *(u32x4*)(xd + kk * 2048) = X[kk];
  }
  if (!LAST) {
    u16* qd = q + (size_t)row * D + cg * 80 + 4 * g;
#pragma unroll
    for (int b = 0; b < 5; ++b) *(u32x2*)(qd + 16 * b) = u32x2{cvtpk(acc[b][0] * QSCALE, acc[b][1] * QSCALE), cvtpk(acc[b][2] * QSCALE, acc[b][3] * QSCALE)};
  } else {
    *(u32x2*)(q + (size_t)row * D + 560 + 4 * g) = u32x2{cvtpk(acc[0][0] * QSCALE, acc[0][1] * QSCALE), cvtpk(acc[0][2] * QSCALE, acc[0][3] * QSCALE)};
    const int tile = tg * 2 + (wid >> 1), s = wid & 1, key = 16 * s + t16;
    char* kb = (char*)kvf + (size_t)tile * 4096;
    *(u32x2*)(kb + ((g >> 1) * 32 + key) * 16 + (g & 1) * 8) = u32x2{cvtpk(acc[1][0], acc[1][1]), cvtpk(acc[1][2], acc[1][3])};
    char* vb = kb + (2 + s) * 1024 + (g & 1) * 512 + (g >> 1) * 8;
    *(u32x2*)(vb + t16 * 16) = u32x2{cvtpk(acc[2][0], acc[2][1]), cvtpk(acc[2][2], acc[2][3])};
    const unsigned cst = (t16 == 2 || t16 == 4) ? 0x3F803F80u : 0u;
    u32x2 e = t16 < 2 ? u32x2{cvtpk(acc[3][0], acc[3][1]), cvtpk(acc[3][2], acc[3][3])} : u32x2{cst, cst};
    *(u32x2*)(vb + (16 + t16) * 16) = e;
    if (g < 2) {
      u32x4 k1 = g == 0 ? u32x4{cvtpk(acc[4][2], acc[4][3]), 0x00003F80u, 0u, 0u} : u32x4{0u, 0u, 0u, 0u};
      *(u32x4*)(kb + 1024 + (g * 32 + key) * 16) = k1;
    }
  }
}
__global__ __launch_bounds__(256) void k_pre(const u16* __restrict__ x, const u16* __restrict__ wq, const u16* __restrict__ wk, const u16* __restrict__ wv,
                                             u16* __restrict__ xnp, u16* __restrict__ q, u16* __restrict__ kvf) {
  __shared__ __attribute__((aligned(16))) char smem[PRE_LDS];
  const int wid = __builtin_amdgcn_readfirstlane(threadIdx.x >> 6), lane = threadIdx.x & 63;
  const int xcd = blockIdx.x & 7, idx = blockIdx.x >> 3;
  const int tg = xcd * 4 + (idx >> 3), cg = idx & 7;
  if (cg < 7) pre_body<false>((LAS char*)smem, x, wq, wk, wv, xnp, q, kvf, tg, cg, wid, lane);
  else pre_body<true>((LAS char*)smem, x, wq, wk, wv, xnp, q, kvf, tg, cg, wid, lane);
}

constexpr float MSHIFT = 80.f;
DI float xhalf_max(float v) { auto rr = __builtin_amdgcn_permlane32_swap(__float_as_uint(v), __float_as_uint(v), false, false); return fmaxf(__uint_as_float(rr[0]), __uint_as_float(rr[1])); }
DI float max16(const f32x16& s) {
  float a = fmaxf(fmaxf(s[0], s[1]), s[2]), b = fmaxf(fmaxf(s[3], s[4]), s[5]);
  a = fmaxf(fmaxf(a, s[6]), s[7]); b = fmaxf(fmaxf(b, s[8]), s[9]);
  a = fmaxf(fmaxf(a, s[10]), s[11]); b = fmaxf(fmaxf(b, s[12]), s[13]);
  a = fmaxf(fmaxf(a, s[14]), s[15]); return fmaxf(a, b);
}
DI void exp_pack(const f32x16& s, bf16x8& p0, bf16x8& p1) {
  u32x4 a, b;
#pragma unroll
  for (int i = 0; i < 4; ++i) a[i] = cvtpk(__builtin_amdgcn_exp2f(s[2 * i]), __builtin_amdgcn_exp2f(s[2 * i + 1]));
#pragma unroll
  for (int i = 0; i < 4; ++i) b[i] = cvtpk(__builtin_amdgcn_exp2f(s[8 + 2 * i]), __builtin_amdgcn_exp2f(s[8 + 2 * i + 1]));
  p0 = __builtin_bit_cast(bf16x8, a); p1 = __builtin_bit_cast(bf16x8, b);
}
DI void store_o(u16* __restrict__ op, int row, int h, int hi, const float* acc  , float rl) {
  const int tb = row >> 5, tok = row & 31;
  u32x4 a = {cvtpk(acc[0] * rl, acc[1] * rl), cvtpk(acc[2] * rl, acc[3] * rl), cvtpk(acc[4] * rl, acc[5] * rl), cvtpk(acc[6] * rl, acc[7] * rl)};
  *(u32x4*)(op + ((size_t)(tb * 36 + h) * 64 + hi * 32 + tok) * 8) = a;
  if (!hi) {
    const int hl = h & 7;
    *(unsigned*)(op + ((size_t)(tb * 36 + 32 + (h >> 3)) * 64 + ((hl >> 1) & 1) * 32 + tok) * 8 + (2 * (hl >> 2) + (hl & 1)) * 2) = cvtpk(acc[8] * rl, acc[9] * rl);
  }
}
DI void prefetch_seg(const char* base, unsigned rowstride, int nchunk, int lane) {
  u32x4 d = {};
  for (int c = lane; c < nchunk; c += 64) {
    const int r = c / 72, k = c - r * 72;
    asm volatile("global_load_dwordx4 %0, %1, %2" : "+v"(d) : "v"((unsigned)(r * rowstride + k * 16)), "s"(base) : "memory");
  }
  asm volatile("s_waitcnt vmcnt(0)" : "+v"(d) :: "memory");
}
template <int T, int NSPLIT, int RG>
__global__ __launch_bounds__(64 * NSPLIT * RG + 64) void k_attn(const u16* __restrict__ q, const u16* __restrict__ kvf, u16* __restrict__ op,
    const u16* __restrict__ w1, const u16* __restrict__ w3, const u16* __restrict__ w2, const u16* __restrict__ wo) {
  constexpr int NT = 64 / NSPLIT;
  __shared__ float red[RG * NSPLIT * T * 12 * 64];
  const int wid = threadIdx.x >> 6, lane = threadIdx.x & 63, hi = lane >> 5, h = lane & 31;
  if (__builtin_amdgcn_readfirstlane(wid) == NSPLIT * RG) {
    const int x = blockIdx.x & 7, idx = blockIdx.x >> 3;
    prefetch_seg((const char*)w1 + (size_t)(18 * idx) * (F * 2) + x * 1152, F * 2, 18 * 72, lane);
    prefetch_seg((const char*)w3 + (size_t)(18 * idx) * (F * 2) + x * 1152, F * 2, 18 * 72, lane);
    if (x < 6) prefetch_seg((const char*)w2 + (size_t)(672 * x + 21 * idx) * 1152, 1152, 21 * 72, lane);
    else if (x == 6) prefetch_seg((const char*)w2 + (size_t)(4032 + 18 * idx) * 1152, 1152, 18 * 72, lane);
    else prefetch_seg((const char*)wo + (size_t)(18 * idx) * 1152, 1152, 18 * 72, lane);
    return;
  }
  const int rg = wid / NSPLIT, sp = wid % NSPLIT;
  const int row0 = (blockIdx.x * RG + rg) * T;
  bf16x8 Q0[T], Q1[T];
#pragma unroll
  for (int t = 0; t < T; ++t) {
    const u16* qp = q + (size_t)(row0 + t) * D + h * DH;
    Q0[t] = *(const bf16x8*)(qp + 8 * hi);
    unsigned w = *(const unsigned*)(qp + 16);
    u32x4 x = {hi ? 0u : w, 0u, 0u, 0u};
    Q1[t] = __builtin_bit_cast(bf16x8, x);
  }
  const bf16x8* kv = (const bf16x8*)kvf + (size_t)sp * NT * 256 + lane;
  bf16x8 K0 = kv[0], K1 = kv[64], V0 = kv[128], V1 = kv[192];
  float mref[T];
  const f32x16 zero = {};
#pragma unroll
  for (int t = 0; t < T; ++t) {
    f32x16 s = MFMA32(K0, Q0[t], zero); s = MFMA32(K1, Q1[t], s);
    float m = xhalf_max(max16(s)) + MSHIFT;
    unsigned mb = (unsigned)f2bf(-m);
    mref[t] = -bf2f((u16)mb);
    u32x4 x = __builtin_bit_cast(u32x4, Q1[t]); x[1] = hi ? 0u : mb; Q1[t] = __builtin_bit_cast(bf16x8, x);
  }
  f32x16 O[T];
#pragma unroll
  for (int t = 0; t < T; ++t) O[t] = zero;
#define TILE_STEP(K0_, K1_, V0_, V1_)                                   \
  _Pragma("unroll") for (int t = 0; t < T; ++t) {                          \
    f32x16 s = MFMA32(K0_, Q0[t], zero); s = MFMA32(K1_, Q1[t], s);        \
    bf16x8 p0, p1; exp_pack(s, p0, p1);                                    \
    O[t] = MFMA32(V0_, p0, O[t]); O[t] = MFMA32(V1_, p1, O[t]);            \
  }
  for (int it = 0; it < NT; it += 2) {
    bf16x8 nK0 = kv[(it + 1) * 256], nK1 = kv[(it + 1) * 256 + 64], nV0 = kv[(it + 1) * 256 + 128], nV1 = kv[(it + 1) * 256 + 192];
    TILE_STEP(K0, K1, V0, V1);
    const int nx = (it + 2 < NT) ? it + 2 : it;
    K0 = kv[nx * 256]; K1 = kv[nx * 256 + 64]; V0 = kv[nx * 256 + 128]; V1 = kv[nx * 256 + 192];
    TILE_STEP(nK0, nK1, nV0, nV1);
  }
  bool bad = false;
#pragma unroll
  for (int t = 0; t < T; ++t) {
    float l = hi ? O[t][8] : O[t][10];
    bad |= !(l < 1e30f);
#pragma unroll
    for (int r = 0; r < 10; ++r) bad |= !(fabsf(O[t][r]) < 1e36f);
  }
  if (__any(bad)) {
#pragma unroll
    for (int t = 0; t < T; ++t) {
      u32x4 x = __builtin_bit_cast(u32x4, Q1[t]); x[1] = 0u; bf16x8 q1 = __builtin_bit_cast(bf16x8, x);
      f32x16 acc = zero; float m = -3.0e38f;
      for (int it = 0; it < NT; ++it) {
        bf16x8 k0 = kv[it * 256], k1 = kv[it * 256 + 64], v0 = kv[it * 256 + 128], v1 = kv[it * 256 + 192];
        f32x16 s = MFMA32(k0, Q0[t], zero); s = MFMA32(k1, q1, s);
        float mn = fmaxf(m, xhalf_max(max16(s)));
        float a = __builtin_amdgcn_exp2f(m - mn); m = mn;
#pragma unroll
        for (int r = 0; r < 16; ++r) { acc[r] *= a; s[r] -= mn; }
        bf16x8 p0, p1; exp_pack(s, p0, p1);
        acc = MFMA32(v0, p0, acc); acc = MFMA32(v1, p1, acc);
      }
      O[t] = acc; mref[t] = m;
    }
  }
#pragma unroll
  for (int t = 0; t < T; ++t) {
    float* dst = red + ((rg * NSPLIT + sp) * T + t) * 12 * 64 + lane;
#pragma unroll
    for (int r = 0; r < 11; ++r) dst[r * 64] = O[t][r];
    dst[11 * 64] = mref[t];
  }
  __syncthreads();
  for (int t = sp; t < T; t += NSPLIT) {
    float mm = -3.0e38f;
    for (int w = 0; w < NSPLIT; ++w) mm = fmaxf(mm, red[((rg * NSPLIT + w) * T + t) * 12 * 64 + 11 * 64 + lane]);
    float acc[11];
    for (int r = 0; r < 11; ++r) acc[r] = 0.f;
    for (int w = 0; w < NSPLIT; ++w) {
      const float* src = red + ((rg * NSPLIT + w) * T + t) * 12 * 64 + lane;
      float f = __builtin_amdgcn_exp2f(src[11 * 64] - mm);
      for (int r = 0; r < 11; ++r) acc[r] += f * src[r * 64];
    }
    float l = hi ? acc[8] : acc[10]; float rl = 1.f / l;
    store_o(op, row0 + t, h, hi, acc, rl);
  }
}

constexpr int STG = 18560;
constexpr int REGB = 9344;
constexpr int RING = 4;
constexpr int LDS_GEMM = STG * RING;

template <int NST, bool NAT, class Src>
DI void gemm_core(LAS char* lds, const Src& src, const char* tptr, f32x16 (&acc)[9], int wid, int lane) {
  const bool weven = (wid & 1) == 0;
  const unsigned ldsbase = (unsigned)(size_t)lds;
  const unsigned ldsw = __builtin_amdgcn_readfirstlane(ldsbase + (wid >> 1) * REGB + (wid & 1) * 5120);
  const int G = lane >> 4, q = (lane & 15) >> 2, p = lane & 3, hi = lane >> 5;
  LAS char* trb = lds + (G & 1) * REGB + ((NAT ? 8 * hi : 4 * hi) + q) * 288 + 8 * p;
  const unsigned toff = lane * 16;
  u32x4 T[NST][2];
  bf16x8 A[NST * 18];
#define ISSUE(j, i) do { if ((j) < NST) { \
    if ((i) < 4) glds(src.voff((j), (i)), ldsw, ((j) % RING) * STG + (i) * 1024, src.wptr(j)); \
    else if ((i) == 4) { if (weven) glds(src.voff((j), 4), ldsw, ((j) % RING) * STG + 4096, src.wptr(j)); } \
    else if ((i) == 5) gload<0>(T[(j) < NST ? (j) : 0][0], toff, tptr + (j) * 2048); \
    else gload<1024>(T[(j) < NST ? (j) : 0][1], toff, tptr + (j) * 2048); } } while (0)
#define RD(g) do { if ((g) < NST * 18) { constexpr int j_ = 0; (void)j_; const int jj = (g) / 18, mm = (g) % 18, ss = mm / 9, bb = mm % 9; \
    A[(g) < NST * 18 ? (g) : 0] = trread(trb, (jj % RING) * STG + (16 * ss) * 288 + 32 * bb, (jj % RING) * STG + (16 * ss + (NAT ? 4 : 8)) * 288 + 32 * bb); } } while (0)
#pragma unroll
  for (int j = 0; j < 3; ++j)
#pragma unroll
    for (int i = 0; i < 7; ++i) ISSUE(j, i);
  asm volatile("s_waitcnt vmcnt(%2)" : "+v"(T[0][0]), "+v"(T[0][1]) : "i"(12) : "memory");
  __builtin_amdgcn_s_barrier();
  SCHED();
  RD(0); RD(1); RD(2);
  SCHED();
#pragma unroll
  for (int j = 0; j < NST; ++j) {
#pragma unroll
    for (int m = 0; m < 18; ++m) {
      const int g = j * 18 + m;
      if (m == 15 && j + 1 < NST) {
        if (j + 2 < NST) asm volatile("s_waitcnt vmcnt(%2)" : "+v"(T[j + 1 < NST ? j + 1 : 0][0]), "+v"(T[j + 1 < NST ? j + 1 : 0][1]) : "i"(6) : "memory");
        else asm volatile("s_waitcnt vmcnt(%2)" : "+v"(T[j + 1 < NST ? j + 1 : 0][0]), "+v"(T[j + 1 < NST ? j + 1 : 0][1]) : "i"(0) : "memory");
        __builtin_amdgcn_s_barrier();
        SCHED();
      }
      RD(g + 3);
      SCHED();
      acc[m % 9] = MFMA32(A[g], __builtin_bit_cast(bf16x8, T[j][m / 9]), acc[m % 9]);
      SCHED();
      if (m >= 15) ISSUE(j + 3, m - 15);
      else if (m < 4 && j >= 1) ISSUE(j + 2, m + 3);
      SCHED();
    }
  }
#undef ISSUE
#undef RD
}

DI void dma_rc(int wid, int lane, int i, int& row, int& c16) {
  const int ee = (wid & 1) * 5 + i;
  const int c = 64 * ee + lane;
  row = c / 18; c16 = c - row * 18;
}

struct SrcPlain {
  const char* base; unsigned stride; unsigned v[5];
  DI const char* wptr(int j) const { return base + (size_t)j * stride; }
  DI unsigned voff(int, int i) const { return v[i]; }
};
struct SrcOproj {
  const char* base; unsigned v[5], v2[5];
  DI const char* wptr(int j) const { return j < 16 ? base + (size_t)j * (36 * D * 2) : base + (size_t)(j - 16) * (288 * D * 2); }
  DI unsigned voff(int j, int i) const { return j < 16 ? v[i] : v2[i]; }
};

__global__ __launch_bounds__(256, 2) void k_gemm1(const u16* __restrict__ xnp, const u16* __restrict__ w1, const u16* __restrict__ w3, u16* __restrict__ hp, unsigned long long* __restrict__ tbuf) {
  __shared__ __attribute__((aligned(16))) char smem[LDS_GEMM];
  TDECL; RSTAMP(4); TSTAMP(0);
  const int wid = __builtin_amdgcn_readfirstlane(threadIdx.x >> 6), lane = threadIdx.x & 63;
  const int xcd = blockIdx.x & 7, idx = blockIdx.x >> 3;
  const int st = 4 * xcd + (idx & 3), tt = idx >> 2;
  const int tb = 4 * tt + wid;
  SrcPlain src;
  src.base = (const char*)((wid >> 1) ? w3 : w1) + st * 288;
  src.stride = 32 * F * 2;
#pragma unroll
  for (int i = 0; i < 5; ++i) { int r, c; dma_rc(wid, lane, i, r, c); src.v[i] = (unsigned)(r * F * 2 + c * 16); }
  f32x16 acc[9];
#pragma unroll
  for (int b = 0; b < 9; ++b) acc[b] = f32x16{};
  gemm_core<18, true>((LAS char*)smem, src, (const char*)xnp + (size_t)tb * 36 * 1024, acc, wid, lane);
  TSTAMP(1);
  char* hdst = (char*)hp + ((size_t)(tb * 288 + st * 9) * 64 + lane) * 16;
#pragma unroll
  for (int b = 0; b < 9; ++b) {
    float h[8];
#pragma unroll
    for (int j = 0; j < 8; ++j) h[j] = silu(acc[b][j]) * acc[b][8 + j];
    u32x4 o = {cvtpk(h[0], h[1]), cvtpk(h[2], h[3]), cvtpk(h[4], h[5]), cvtpk(h[6], h[7])};
    *(u32x4*)(hdst + b * 1024) = o;
  }
  TSTAMP(2); RSTAMP(5); TFLUSH();
}

DI void store_part(u16* __restrict__ P, int slot, int tb, int nh, int lane, const f32x16 (&acc)[9]) {
  char* dst = (char*)P + ((size_t)((slot * 64 + tb) * 18 + nh * 9) * 2 * 64 + lane) * 16;
#pragma unroll
  for (int b = 0; b < 9; ++b) {
    u32x4 o0 = {cvtpk(acc[b][0], acc[b][1]), cvtpk(acc[b][2], acc[b][3]), cvtpk(acc[b][4], acc[b][5]), cvtpk(acc[b][6], acc[b][7])};
    u32x4 o1 = {cvtpk(acc[b][8], acc[b][9]), cvtpk(acc[b][10], acc[b][11]), cvtpk(acc[b][12], acc[b][13]), cvtpk(acc[b][14], acc[b][15])};
    *(u32x4*)(dst + b * 2048) = o0;
    *(u32x4*)(dst + b * 2048 + 1024) = o1;
  }
}
__global__ __launch_bounds__(256, 2) void k_gemm2(const u16* __restrict__ hp, const u16* __restrict__ w2, const u16* __restrict__ op, const u16* __restrict__ wo, u16* __restrict__ P, unsigned long long* __restrict__ tbuf) {
  __shared__ __attribute__((aligned(16))) char smem[LDS_GEMM];
  TDECL; RSTAMP(4); TSTAMP(0);
  const int wid = __builtin_amdgcn_readfirstlane(threadIdx.x >> 6), lane = threadIdx.x & 63;
  const int xcd = blockIdx.x & 7, idx = blockIdx.x >> 3;
  const int nh = idx & 1, tt = idx >> 1;
  const int tb = 4 * tt + wid;
  const int col0 = nh * 288 + (wid >> 1) * 144;
  f32x16 acc[9];
#pragma unroll
  for (int b = 0; b < 9; ++b) acc[b] = f32x16{};
  if (xcd < 7) {
    SrcPlain src;
    const int f0 = 672 * xcd;
    src.base = (const char*)w2 + ((size_t)f0 * D + col0) * 2;
    src.stride = 32 * D * 2;
#pragma unroll
    for (int i = 0; i < 5; ++i) { int r, c; dma_rc(wid, lane, i, r, c); src.v[i] = (unsigned)(r * D * 2 + c * 16); }
    const char* tp = (const char*)hp + ((size_t)tb * 288 + (f0 >> 4)) * 1024;
    if (xcd < 6) gemm_core<21, false>((LAS char*)smem, src, tp, acc, wid, lane);
    else gemm_core<18, false>((LAS char*)smem, src, tp, acc, wid, lane);
  } else {
    SrcOproj src;
    src.base = (const char*)wo + col0 * 2;
#pragma unroll
    for (int i = 0; i < 5; ++i) {
      int r, c; dma_rc(wid, lane, i, r, c);
      src.v[i] = (unsigned)((18 * (r >> 4) + (r & 15)) * D * 2 + c * 16);
      src.v2[i] = (unsigned)((144 * (r >> 4) + 18 * ((r & 15) >> 1) + 16 + (r & 1)) * D * 2 + c * 16);
    }
    gemm_core<18, false>((LAS char*)smem, src, (const char*)op + (size_t)tb * 36 * 1024, acc, wid, lane);
  }
  TSTAMP(1);
  store_part(P, xcd, tb, nh, lane, acc);
  TSTAMP(2); RSTAMP(5); TFLUSH();
}

__global__ __launch_bounds__(576) void k_final(const u16* __restrict__ P, const u16* __restrict__ x, u16* __restrict__ out) {
  __shared__ float tile[32][148];
  const int tb = blockIdx.x >> 2, Q = blockIdx.x & 3, nh = Q >> 1, half = Q & 1;
  const int b = threadIdx.x >> 6, lane = threadIdx.x & 63, hi = lane >> 5, tok = lane & 31;
  const int r = threadIdx.x / 18, c8 = threadIdx.x - r * 18;
  const size_t goff = (size_t)(tb * 32 + r) * D + Q * 144 + c8 * 8;
  u32x4 xv = *(const u32x4*)(x + goff);
  u32x4 pv[8];
#pragma unroll
  for (int s = 0; s < 8; ++s) pv[s] = *(const u32x4*)((const char*)P + ((size_t)(((s * 64 + tb) * 18 + nh * 9 + b) * 2 + half) * 64 + lane) * 16);
  float v[8];
#pragma unroll
  for (int j = 0; j < 8; ++j) {
    float f = 0.f;
#pragma unroll
    for (int s = 0; s < 7; ++s) { unsigned w = pv[s][j >> 1]; f += __uint_as_float((j & 1) ? (w & 0xffff0000u) : (w << 16)); }
    unsigned w = pv[7][j >> 1]; float sv = __uint_as_float((j & 1) ? (w & 0xffff0000u) : (w << 16));
    v[j] = sv * silu(f);
  }
  *(f32x4*)&tile[tok][16 * b + 4 * hi] = f32x4{v[0], v[1], v[2], v[3]};
  *(f32x4*)&tile[tok][16 * b + 8 + 4 * hi] = f32x4{v[4], v[5], v[6], v[7]};
  __syncthreads();
  f32x4 a0 = *(const f32x4*)&tile[r][c8 * 8], a1 = *(const f32x4*)&tile[r][c8 * 8 + 4];
  float o[8];
#pragma unroll
  for (int j = 0; j < 8; ++j) { unsigned w = xv[j >> 1]; float xx = __uint_as_float((j & 1) ? (w & 0xffff0000u) : (w << 16)); o[j] = xx + (j < 4 ? a0[j] : a1[j - 4]); }
  u32x4 ov = {cvtpk(o[0], o[1]), cvtpk(o[2], o[3]), cvtpk(o[4], o[5]), cvtpk(o[6], o[7])};
  *(u32x4*)(out + goff) = ov;
}

#define AT_T 4
#define AT_NS 4
#define AT_RG 2

extern "C" void kernel_launch(void* const* d_in, const int* in_sizes, int n_in, void* d_out, int out_size, void* d_ws, size_t ws_size, hipStream_t stream) {
  const u16* x = (const u16*)d_in[0];
  const u16* wq = (const u16*)d_in[1];
  const u16* wk = (const u16*)d_in[2];
  const u16* wv = (const u16*)d_in[3];
  const u16* wo = (const u16*)d_in[4];
  const u16* w1 = (const u16*)d_in[6];
  const u16* w3 = (const u16*)d_in[7];
  const u16* w2 = (const u16*)d_in[8];
  u16* out = (u16*)d_out;
  u16* ws = (u16*)d_ws;
  u16* xn = ws;
  u16* q = xn + S * D;
  u16* k = q + S * D;
  u16* v = k + S * 32;
  u16* op = v + S * 32;
  u16* xnp = op + S * D;
  u16* hp = xnp + S * D;
  u16* P = hp + S * F;
  u16* kvf = P + 8 * S * D;
  unsigned long long* tb1 = (unsigned long long*)((char*)d_ws + (200u << 20));
  unsigned long long* tb2 = (unsigned long long*)((char*)d_ws + (201u << 20));
  k_pre<<<256, 256, 0, stream>>>(x, wq, wk, wv, xnp, q, kvf);
  k_attn<AT_T, AT_NS, AT_RG><<<S / (AT_T * AT_RG), 64 * AT_NS * AT_RG + 64, 0, stream>>>(q, kvf, op, w1, w3, w2, wo);
  k_gemm1<<<512, 256, 0, stream>>>(xnp, w1, w3, hp, tb1);
  k_gemm2<<<256, 256, 0, stream>>>(hp, w2, op, wo, P, tb2);
  k_final<<<256, 576, 0, stream>>>(P, x, out);
}
```

```cpp
#define NPF 1
#include <hip/hip_runtime.h>
#include <hip/hip_bf16.h>
#include <stdint.h>

#define PF 3
typedef unsigned short u16;
using bf16x8 = __attribute__((ext_vector_type(8))) short;
using v4s = __attribute__((ext_vector_type(4))) short;
using f32x4 = __attribute__((ext_vector_type(4))) float;
using f32x16 = __attribute__((ext_vector_type(16))) float;
using u32x4 = __attribute__((ext_vector_type(4))) unsigned;
using u32x2 = __attribute__((ext_vector_type(2))) unsigned;
#define DI __device__ __forceinline__
#define LAS __attribute__((address_space(3)))
#define MFMA32(a, b, c) __builtin_amdgcn_mfma_f32_32x32x16_bf16((a), (b), (c), 0, 0, 0)
#define SCHED() __builtin_amdgcn_sched_barrier(0)

constexpr int S = 2048, D = 576, H = 32, DH = 18, F = 4608;
constexpr float QSCALE = 0.23570226039551584f * 1.4426950408889634f;
constexpr float LOG2E = 1.4426950408889634f;

DI float bf2f(u16 v) { return __uint_as_float(((unsigned)v) << 16); }
DI u16 f2bf(float x) { unsigned u = __float_as_uint(x); u += 0x7fffu + ((u >> 16) & 1u); return (u16)(u >> 16); }
using f32x2 = __attribute__((ext_vector_type(2))) float;
using bf2 = __attribute__((ext_vector_type(2))) __bf16;
DI unsigned cvtpk(float lo, float hi) { f32x2 v = {lo, hi}; return __builtin_bit_cast(unsigned, __builtin_convertvector(v, bf2)); }
DI float silu(float a) { return a * __builtin_amdgcn_rcpf(1.f + __builtin_amdgcn_exp2f(-LOG2E * a)); }

#define TDECL
#define TSTAMP(i)
#define RSTAMP(i)
#define TFLUSH()

#define MFMA16(a, b, c) __builtin_amdgcn_mfma_f32_16x16x32_bf16((a), (b), (c), 0, 0, 0)
typedef __attribute__((ext_vector_type(4))) short v4i16_t;
DI bf16x8 trread(LAS char* p, int imm0, int imm1) {
  v4i16_t a = __builtin_amdgcn_ds_read_tr16_b64_v4i16((LAS v4i16_t*)(p + imm0));
  v4i16_t b = __builtin_amdgcn_ds_read_tr16_b64_v4i16((LAS v4i16_t*)(p + imm1));
  return __builtin_shufflevector(a, b, 0, 1, 2, 3, 4, 5, 6, 7);
}
DI void glds(unsigned voff, unsigned ldsw, int imm, const char* g) {
  asm volatile("s_add_u32 m0, %1, %2\n\ts_nop 0\n\tglobal_load_lds_dwordx4 %0, %3" ::"v"(voff), "s"(ldsw), "i"(imm), "s"(g) : "memory", "scc");
}
template <int OFF>
DI void gload(u32x4& f, unsigned voff, const char* g) {
  asm volatile("global_load_dwordx4 %0, %1, %2 offset:%3" : "=v"(f) : "v"(voff), "s"(g), "i"(OFF) : "memory");
}
template <int OFF>
DI void gloads(u32x4& f, unsigned voff, const char* g) {
  asm volatile("global_load_dwordx4 %0, %1, %2 offset:%3 sc1" : "=v"(f) : "v"(voff), "s"(g), "i"(OFF) : "memory");
}
constexpr float MSHIFT = 100.f;
DI float xhalf_max(float v) { auto rr = __builtin_amdgcn_permlane32_swap(__float_as_uint(v), __float_as_uint(v), false, false); return fmaxf(__uint_as_float(rr[0]), __uint_as_float(rr[1])); }
DI float max16(const f32x16& s) {
  float a = fmaxf(fmaxf(s[0], s[1]), s[2]), b = fmaxf(fmaxf(s[3], s[4]), s[5]);
  a = fmaxf(fmaxf(a, s[6]), s[7]); b = fmaxf(fmaxf(b, s[8]), s[9]);
  a = fmaxf(fmaxf(a, s[10]), s[11]); b = fmaxf(fmaxf(b, s[12]), s[13]);
  a = fmaxf(fmaxf(a, s[14]), s[15]); return fmaxf(a, b);
}
DI void exp_pack(const f32x16& s, bf16x8& p0, bf16x8& p1) {
  u32x4 a, b;
#pragma unroll
  for (int i = 0; i < 4; ++i) a[i] = cvtpk(__builtin_amdgcn_exp2f(s[2 * i]), __builtin_amdgcn_exp2f(s[2 * i + 1]));
#pragma unroll
  for (int i = 0; i < 4; ++i) b[i] = cvtpk(__builtin_amdgcn_exp2f(s[8 + 2 * i]), __builtin_amdgcn_exp2f(s[8 + 2 * i + 1]));
  p0 = __builtin_bit_cast(bf16x8, a); p1 = __builtin_bit_cast(bf16x8, b);
}
#define V10(x) "v" #x "0", "v" #x "1", "v" #x "2", "v" #x "3", "v" #x "4", "v" #x "5", "v" #x "6", "v" #x "7", "v" #x "8", "v" #x "9"
#define A10(x) "a" #x "0", "a" #x "1", "a" #x "2", "a" #x "3", "a" #x "4", "a" #x "5", "a" #x "6", "a" #x "7", "a" #x "8", "a" #x "9"
#define S10(x) "s" #x "0", "s" #x "1", "s" #x "2", "s" #x "3", "s" #x "4", "s" #x "5", "s" #x "6", "s" #x "7", "s" #x "8", "s" #x "9"
#define VALL V10(), V10(1), V10(2), V10(3), V10(4), V10(5), V10(6), V10(7), V10(8), V10(9), V10(10), V10(11), V10(12), V10(13), V10(14), V10(15), V10(16), V10(17), V10(18), V10(19), V10(20), V10(21), V10(22), V10(23), V10(24), "v250", "v251", "v252", "v253", "v254", "v255"
#define AALL A10(), A10(1), A10(2), A10(3), A10(4), A10(5), A10(6), A10(7), A10(8), A10(9), A10(10), A10(11), A10(12), A10(13), A10(14), A10(15), A10(16), A10(17), A10(18), A10(19), A10(20), A10(21), A10(22), A10(23), A10(24), "a250", "a251", "a252", "a253", "a254", "a255"

template <int OFF>
DI void gload1(unsigned& f, unsigned voff, const char* g) {
  asm volatile("global_load_dword %0, %1, %2 offset:%3" : "=v"(f) : "v"(voff), "s"(g), "i"(OFF) : "memory");
}
template <int LOFF, int GOFF>
DI void glds2(unsigned voff, unsigned ldsw, const char* g) {
  asm volatile("s_add_u32 m0, %1, %2\n\ts_nop 0\n\tglobal_load_lds_dwordx4 %0, %3 offset:%4" ::"v"(voff), "s"(ldsw), "i"(LOFF - GOFF), "s"(g), "i"(GOFF) : "memory", "scc");
}
constexpr int WO_R2 = 110592;
template <int J> DI void wo_row9(unsigned voff, unsigned ldsw, const char* g) {
  glds2<0 * 1024, 0 * 128>(voff, ldsw, g); glds2<1 * 1024, 1 * 128>(voff, ldsw, g); glds2<2 * 1024, 2 * 128>(voff, ldsw, g);
  glds2<3 * 1024, 3 * 128>(voff, ldsw, g); glds2<4 * 1024, 4 * 128>(voff, ldsw, g); glds2<5 * 1024, 5 * 128>(voff, ldsw, g);
  glds2<6 * 1024, 6 * 128>(voff, ldsw, g); glds2<7 * 1024, 7 * 128>(voff, ldsw, g); glds2<8 * 1024, 8 * 128>(voff, ldsw, g);
}
#define EST2 0
#define FMAP 1
#define PFMODE 1
typedef __attribute__((address_space(1))) unsigned long long gu64;
typedef __attribute__((address_space(1))) unsigned gu32;
constexpr unsigned SPINCAP = 1u << 16;
#define BAR() do { __builtin_amdgcn_s_barrier(); asm volatile("" ::: "memory"); SCHED(); } while (0)
#define ACCPIN(w_) asm volatile(w_ : "+v"(acc[0]), "+v"(acc[1]), "+v"(acc[2]), "+v"(acc[3]), "+v"(acc[4]), "+v"(acc[5]), "+v"(acc[6]), "+v"(acc[7]), "+v"(acc[8]) :: "memory")
DI void st64(char* p, u32x2 v) { __hip_atomic_store((gu64*)p, (unsigned long long)v[0] | ((unsigned long long)v[1] << 32), __ATOMIC_RELAXED, __HIP_MEMORY_SCOPE_AGENT); }
DI unsigned wq_voff(int c) { const int r = c / 18, c16 = c - 18 * r, k = 8 * ((r >> 2) & 3) + 4 * (r >> 4) + (r & 3); return (unsigned)(k * 1152 + c16 * 16); }
template <int K0, int K1>
DI void q_steps(LAS char* trb, const u32x4 (&X)[18], f32x4 (&acc)[9]) {
#pragma unroll
  for (int kk = K0; kk < K1; ++kk) {
    const int sl = (kk == 17 ? 0 : kk) * 9216;
    const bf16x8 xf = __builtin_bit_cast(bf16x8, X[kk]);
#pragma unroll
    for (int b = 0; b < 9; ++b) acc[b] = MFMA16(trread(trb, sl + 32 * b, sl + 4608 + 32 * b), xf, acc[b]);
  }
}
template <bool PROD>
DI unsigned att_pre(LAS char* lds, const u16* __restrict__ x, const u16* __restrict__ wq, const u16* __restrict__ wk, const u16* __restrict__ wv,
                    u16* __restrict__ kvf, const u16* __restrict__ wo, char* __restrict__ fl, int ab, int tg, int hg, int wid, int lane, u32x4 (&KK)[4]) {
  const int t16 = lane & 15, g = lane >> 4, hh = wid >> 1, tbk = wid & 1;
  const unsigned ldsb = (unsigned)(size_t)lds;
  unsigned ep;
  { const unsigned zo = 0; asm volatile("global_load_dword %0, %1, %2 sc1" : "=v"(ep) : "v"(zo), "s"(fl) : "memory"); }
  u32x4 X[18];
  {
    const unsigned xoff = (unsigned)((tg * 64 + wid * 16 + (lane >> 3)) * 1152 + (((lane & 7) ^ (lane >> 3)) * 16));
    wo_row9<0>(xoff, __builtin_amdgcn_readfirstlane(ldsb + (8 + wid) * 9216), (const char*)x);
    wo_row9<0>(xoff, __builtin_amdgcn_readfirstlane(ldsb + (12 + wid) * 9216), (const char*)x + 9216);
  }
  u32x4 Wa[5], Wb[5]; unsigned Wc[5];
  unsigned vo[9], vs[3], ls[3];
#pragma unroll
  for (int i = 0; i < 9; ++i) vo[i] = wq_voff(64 * i + lane);
#pragma unroll
  for (int j = 0; j < 3; ++j) { const int i = j < 2 ? wid + 4 * j : 8; vs[j] = wq_voff(64 * i + lane); ls[j] = __builtin_amdgcn_readfirstlane(ldsb + i * 1024); }
  const char* wqb = (const char*)wq + hg * 288;
  const unsigned lw = __builtin_amdgcn_readfirstlane(ldsb + wid * 9216);
  const char* gw = wqb + wid * 36864;
#pragma unroll
  for (int m = 0; m < 2; ++m) {
#pragma unroll
    for (int i = 0; i < 9; ++i) glds(vo[i], lw, m * 36864 + i * 1024, gw + m * 147456);
  }
  asm volatile("s_waitcnt vmcnt(18)" : "+v"(ep) :: "memory");
  {
    const int r = t16 & 7;
    LAS char* xr = lds + (8 + wid + 4 * (t16 >> 3)) * 9216 + r * 128;
    LAS char* xe = xr + ((g ^ r) * 16);
    LAS char* xo = xr + (((4 + g) ^ r) * 16);
#pragma unroll
    for (int kk = 0; kk < 18; ++kk) X[kk] = *(LAS u32x4*)(((kk & 1) ? xo : xe) + (kk >> 1) * 1024);
  }
  asm volatile("s_waitcnt lgkmcnt(0)" : "+v"(X[0]), "+v"(X[1]), "+v"(X[2]), "+v"(X[3]), "+v"(X[4]), "+v"(X[5]), "+v"(X[6]), "+v"(X[7]), "+v"(X[8]) :: "memory");
  asm volatile("" : "+v"(X[9]), "+v"(X[10]), "+v"(X[11]), "+v"(X[12]), "+v"(X[13]), "+v"(X[14]), "+v"(X[15]), "+v"(X[16]), "+v"(X[17]) :: "memory");
  if (PROD) {
    const char* wb = (const char*)(hh ? wv : wk);
#pragma unroll
    for (int it = 0; it < 5; ++it) {
      const int r0 = tbk * 64 + lane + 128 * it, r = r0 < 575 ? r0 : 575;
      gload<0>(Wa[it], r * 36, wb); gload<16>(Wb[it], r * 36, wb); gload1<32>(Wc[it], r * 36, wb);
    }
  }
  {
    const u32x4 onesu = {0x3F803F80u, 0x3F803F80u, 0x3F803F80u, 0x3F803F80u};
    const bf16x8 ones = __builtin_bit_cast(bf16x8, onesu);
    f32x4 s4 = {}, g4 = {};
#pragma unroll
    for (int kk = 0; kk < 18; ++kk) {
      const bf16x8 xf = __builtin_bit_cast(bf16x8, X[kk]);
      s4 = MFMA16(ones, xf, s4);
      g4 = MFMA16(xf, xf, g4);
    }
    const int l3 = lane & 3;
    const float dsel = l3 == 0 ? g4[0] : (l3 == 1 ? g4[1] : (l3 == 2 ? g4[2] : g4[3]));
    const float ss = __shfl(dsel, t16 + 16 * (t16 >> 2));
    const float mean = s4[0] * (1.f / D);
    const float var = fmaxf(ss * (1.f / D) - mean * mean, 0.f);
    const float rstd = __builtin_amdgcn_rsqf(var + 1e-5f);
    const float nb = -mean * rstd;
#pragma unroll
    for (int kk = 0; kk < 18; ++kk) {
#pragma unroll
      for (int i = 0; i < 4; ++i) {
        const unsigned w = X[kk][i];
        X[kk][i] = cvtpk(fmaf(__uint_as_float(w << 16), rstd, nb), fmaf(__uint_as_float(w & 0xffff0000u), rstd, nb));
      }
      asm volatile("" : "+v"(X[kk]) :: "memory");
      if (PROD) { if (kk < 9) glds(vo[kk], lw, 2 * 36864 + kk * 1024, gw + 2 * 147456); }
      else glds(vo[kk % 9], lw, (2 + kk / 9) * 36864 + (kk % 9) * 1024, gw + (2 + kk / 9) * 147456);
    }
    if (!PROD) {
#pragma unroll
      for (int j = 0; j < 3; ++j) glds(vs[j], ls[j], 16 * 9216, wqb + 16 * 36864);
    }
  }
  asm volatile("" : "+v"(X[0]), "+v"(X[1]), "+v"(X[2]), "+v"(X[3]), "+v"(X[4]), "+v"(X[5]), "+v"(X[6]), "+v"(X[7]), "+v"(X[8]) :: "memory");
  asm volatile("" : "+v"(X[9]), "+v"(X[10]), "+v"(X[11]), "+v"(X[12]), "+v"(X[13]), "+v"(X[14]), "+v"(X[15]), "+v"(X[16]), "+v"(X[17]) :: "memory");
  if (PROD) {
    BAR();
    asm volatile("s_waitcnt vmcnt(9)" : "+v"(Wa[0]), "+v"(Wa[1]), "+v"(Wa[2]), "+v"(Wa[3]), "+v"(Wa[4]), "+v"(Wb[0]), "+v"(Wb[1]), "+v"(Wb[2]), "+v"(Wb[3]), "+v"(Wb[4]) :: "memory");
    asm volatile("" : "+v"(Wc[0]), "+v"(Wc[1]), "+v"(Wc[2]), "+v"(Wc[3]), "+v"(Wc[4]) :: "memory");
#pragma unroll
    for (int it = 0; it < 5; ++it) {
      const int r0 = tbk * 64 + lane + 128 * it, k = r0 < 575 ? r0 : 575;
      const int r = (k & ~31) + 16 * ((k >> 2) & 1) + 4 * ((k >> 3) & 3) + (k & 3);
      LAS char* d = lds + 110592 + r * 80;
      *(LAS u32x4*)(d + 32 * hh) = Wa[it]; *(LAS u32x4*)(d + 32 * hh + 16) = Wb[it]; *(LAS unsigned*)(d + 68 - 4 * hh) = Wc[it];
    }
    asm volatile("s_waitcnt vmcnt(9) lgkmcnt(0)" ::: "memory");
  } else {
    asm volatile("s_waitcnt vmcnt(21)" ::: "memory");
  }
  BAR();
  const unsigned long long tag = (unsigned long long)(ep + 1u) | ((unsigned long long)(~(ep + 1u)) << 32);
  if (wid == 0 && lane == 0) __hip_atomic_store((gu64*)(fl + 256 + ab * 8), tag, __ATOMIC_RELAXED, __HIP_MEMORY_SCOPE_AGENT);
  if (PROD) {
    LAS char* tk = lds + 110592 + (4 * g + (t16 >> 2)) * 80 + 8 * (lane & 3);
    f32x4 a1 = {}, a2 = {}, a3 = {}, a4 = {};
#pragma unroll
    for (int kk = 0; kk < 18; ++kk) {
      const bf16x8 xf = __builtin_bit_cast(bf16x8, X[kk]);
      a1 = MFMA16(trread(tk, kk * 2560, kk * 2560 + 1280), xf, a1);
      a2 = MFMA16(xf, trread(tk, kk * 2560 + 32, kk * 2560 + 1280 + 32), a2);
      const bf16x8 w3 = trread(tk, kk * 2560 + 64, kk * 2560 + 1280 + 64);
      a3 = MFMA16(xf, w3, a3);
      a4 = MFMA16(w3, xf, a4);
    }
    const int tile = tg * 2 + (wid >> 1), s = wid & 1, key = 16 * s + t16;
    char* kb = (char*)kvf + (size_t)tile * 4096;
    st64(kb + ((g >> 1) * 32 + key) * 16 + (g & 1) * 8, u32x2{cvtpk(a1[0], a1[1]), cvtpk(a1[2], a1[3])});
    char* vb = kb + (2 + s) * 1024 + (g & 1) * 512 + (g >> 1) * 8;
    st64(vb + t16 * 16, u32x2{cvtpk(a2[0], a2[1]), cvtpk(a2[2], a2[3])});
    const unsigned cst = (t16 == 2 || t16 == 4) ? 0x3F803F80u : 0u;
    st64(vb + (16 + t16) * 16, t16 < 2 ? u32x2{cvtpk(a3[0], a3[1]), cvtpk(a3[2], a3[3])} : u32x2{cst, cst});
    if (g < 2) {
      char* kd = kb + 1024 + (g * 32 + key) * 16;
      st64(kd, g == 0 ? u32x2{cvtpk(a4[2], a4[3]), 0x00003F80u} : u32x2{0u, 0u});
      st64(kd + 8, u32x2{0u, 0u});
    }
  }
  f32x4 acc[9];
#pragma unroll
  for (int b = 0; b < 9; ++b) acc[b] = f32x4{};
  LAS char* trb = lds + (4 * g + (t16 >> 2)) * 288 + 8 * (lane & 3);
  const unsigned fo = ((lane & 31) * 4 + hg) * 128 + 2048;
  unsigned long long fv;
  if (PROD) {
    asm volatile("s_waitcnt lgkmcnt(0)" ::: "memory");
    BAR();
#pragma unroll
    for (int i = 0; i < 9; ++i) glds(vo[i], lw, 12 * 9216 + i * 1024, gw + 12 * 36864);
#pragma unroll
    for (int j = 0; j < 3; ++j) glds(vs[j], ls[j], 16 * 9216, wqb + 16 * 36864);
    q_steps<0, 8>(trb, X, acc);
    ACCPIN("s_waitcnt vmcnt(12) lgkmcnt(0)");
    BAR();
    if (wid == 0 && lane < 4) __hip_atomic_store((gu64*)(fl + 2048 + (tg * 4 + lane) * 128), tag, __ATOMIC_RELAXED, __HIP_MEMORY_SCOPE_AGENT);
#pragma unroll
    for (int j = 0; j < 3; ++j) glds(vs[j], ls[j], 0, wqb + 17 * 36864);
    q_steps<8, 12>(trb, X, acc);
    ACCPIN("s_waitcnt vmcnt(0) lgkmcnt(0)");
    BAR();
    q_steps<12, 14>(trb, X, acc);
    ACCPIN("");
    asm volatile("global_load_dwordx2 %0, %1, %2 sc1" : "=v"(fv) : "v"(fo), "s"(fl) : "memory");
    q_steps<14, 18>(trb, X, acc);
  } else {
    q_steps<0, 8>(trb, X, acc);
    ACCPIN("s_waitcnt vmcnt(0) lgkmcnt(0)");
    BAR();
#pragma unroll
    for (int j = 0; j < 3; ++j) glds(vs[j], ls[j], 0, wqb + 17 * 36864);
    q_steps<8, 14>(trb, X, acc);
    ACCPIN("");
    asm volatile("global_load_dwordx2 %0, %1, %2 sc1" : "=v"(fv) : "v"(fo), "s"(fl) : "memory");
    q_steps<14, 17>(trb, X, acc);
    ACCPIN("s_waitcnt vmcnt(1) lgkmcnt(0)");
    BAR();
    q_steps<17, 18>(trb, X, acc);
  }
  ACCPIN("s_waitcnt lgkmcnt(0)");
  BAR();
  if (PROD) {
    asm volatile("s_waitcnt vmcnt(0)" : "+v"(fv) :: "memory");
    {
      unsigned spins = 0;
      while (!__all(fv == tag)) {
        if (++spins > SPINCAP) break;
        __builtin_amdgcn_s_sleep(1);
        fv = __hip_atomic_load((gu64*)(fl + fo), __ATOMIC_RELAXED, __HIP_MEMORY_SCOPE_AGENT);
      }
    }
    asm volatile("" ::: "memory");
    gloads<0>(KK[0], lane * 16, (const char*)kvf); gloads<1024>(KK[1], lane * 16, (const char*)kvf);
    gloads<0>(KK[2], lane * 16 + 4096, (const char*)kvf); gloads<1024>(KK[3], lane * 16 + 4096, (const char*)kvf);
  {
    LAS char* qx = lds + (16 * wid + t16) * 400;
#pragma unroll
    for (int b = 0; b < 9; ++b) {
#pragma unroll
      for (int u = 0; u < 2; ++u) {
        const int c = 16 * b + 4 * g + 2 * u;
        *(LAS unsigned*)(qx + 2 * c + 12 * ((c * 57) >> 10)) = cvtpk(acc[b][2 * u] * QSCALE, acc[b][2 * u + 1] * QSCALE);
      }
    }
  }
  {
    const int rho = lane >> 3, tt = wid & 1;
    const unsigned cswz = ((lane & 7) ^ (4 * ((rho >> 1) & 1))) * 16;
    const unsigned vmain = rho * 1152 + cswz;
    const unsigned vex = (18 * (4 * (rho >> 2) + 2 * tt + ((rho & 3) >> 1)) + 16 + (rho & 1)) * 1152 + cswz;
    const char* wob = (const char*)wo + (unsigned)__builtin_amdgcn_readfirstlane(18 * 8 * hg * 1152);
    {
    const unsigned vo = hh ? vex : vmain;
    const char* g = wob + (unsigned)__builtin_amdgcn_readfirstlane(hh ? 0 : (18 * 7 + 8 * tt) * 1152);
    const unsigned lw = __builtin_amdgcn_readfirstlane(ldsb + WO_R2 + wid * 9216 * 1);
    wo_row9<0>(vo, lw, g);
#pragma unroll
    for (int m = 1; m < 3; ++m) {
      const int ks = 2 * m + hh;
      const unsigned lw2 = __builtin_amdgcn_readfirstlane(ldsb + (4 * m + wid) * 9216);
      wo_row9<0>(vmain, lw2, wob + (unsigned)__builtin_amdgcn_readfirstlane((18 * ks + 8 * tt) * 1152));
    }
  }
  }
    asm volatile("s_waitcnt vmcnt(18) lgkmcnt(0)" : "+v"(KK[0]), "+v"(KK[1]), "+v"(KK[2]), "+v"(KK[3]) :: "memory");
    BAR();
  } else {
  {
    const int rho = lane >> 3, tt = wid & 1;
    const unsigned cswz = ((lane & 7) ^ (4 * ((rho >> 1) & 1))) * 16;
    const unsigned vmain = rho * 1152 + cswz;
    const unsigned vex = (18 * (4 * (rho >> 2) + 2 * tt + ((rho & 3) >> 1)) + 16 + (rho & 1)) * 1152 + cswz;
    const char* wob = (const char*)wo + (unsigned)__builtin_amdgcn_readfirstlane(18 * 8 * hg * 1152);
    {
    const unsigned vo = hh ? vex : vmain;
    const char* g = wob + (unsigned)__builtin_amdgcn_readfirstlane(hh ? 0 : (18 * 7 + 8 * tt) * 1152);
    const unsigned lw = __builtin_amdgcn_readfirstlane(ldsb + WO_R2 + wid * 9216 * 1);
    wo_row9<0>(vo, lw, g);
#pragma unroll
    for (int m = 1; m < 3; ++m) {
      const int ks = 2 * m + hh;
      const unsigned lw2 = __builtin_amdgcn_readfirstlane(ldsb + (4 * m + wid) * 9216);
      wo_row9<0>(vmain, lw2, wob + (unsigned)__builtin_amdgcn_readfirstlane((18 * ks + 8 * tt) * 1152));
    }
  }
  }
  {
    LAS char* qx = lds + (16 * wid + t16) * 400;
#pragma unroll
    for (int b = 0; b < 9; ++b) {
#pragma unroll
      for (int u = 0; u < 2; ++u) {
        const int c = 16 * b + 4 * g + 2 * u;
        *(LAS unsigned*)(qx + 2 * c + 12 * ((c * 57) >> 10)) = cvtpk(acc[b][2 * u] * QSCALE, acc[b][2 * u + 1] * QSCALE);
      }
    }
  }
    asm volatile("s_waitcnt vmcnt(18) lgkmcnt(0)" : "+v"(fv) :: "memory");
    if (wid == 0) {
      unsigned spins = 0;
      while (!__all(fv == tag)) {
        if (++spins > SPINCAP) break;
        __builtin_amdgcn_s_sleep(1);
        fv = __hip_atomic_load((gu64*)(fl + fo), __ATOMIC_RELAXED, __HIP_MEMORY_SCOPE_AGENT);
      }
    }
    BAR();
    gloads<0>(KK[0], lane * 16, (const char*)kvf); gloads<1024>(KK[1], lane * 16, (const char*)kvf);
    gloads<0>(KK[2], lane * 16 + 4096, (const char*)kvf); gloads<1024>(KK[3], lane * 16 + 4096, (const char*)kvf);
    asm volatile("s_waitcnt vmcnt(0)" : "+v"(KK[0]), "+v"(KK[1]), "+v"(KK[2]), "+v"(KK[3]) :: "memory");
  }
  return ep;
}
DI void attn_wg(LAS char* lds, int ab, const u16* __restrict__ x, const u16* __restrict__ wq, const u16* __restrict__ wk, const u16* __restrict__ wv,
                u16* __restrict__ kvf, const u16* __restrict__ wo, u16* __restrict__ P, char* __restrict__ fl,
                const u16* __restrict__ w1, const u16* __restrict__ w3, const u16* __restrict__ w2,
                int wid, int lane) {
  const int xcd = ab & 7, ii = ab >> 3, hg = xcd & 3, tg = (xcd >> 2) * 16 + ii;
  const bool prod = (ii & 3) == hg;
  const int tbk = wid & 1, hh = wid >> 1, hi = lane >> 5, tok = lane & 31;
  const int tb = 2 * tg + tbk;
  u32x4 KK[4];
  const unsigned ep = prod ? att_pre<true>(lds, x, wq, wk, wv, kvf, wo, fl, ab, tg, hg, wid, lane, KK) : att_pre<false>(lds, x, wq, wk, wv, kvf, wo, fl, ab, tg, hg, wid, lane, KK);
  const unsigned long long tag = (unsigned long long)(ep + 1u) | ((unsigned long long)(~(ep + 1u)) << 32);
  unsigned pf[NPF];
  {
    constexpr int CPB = 9 / NPF;
    const int wv_ = prod ? 0 : (xcd >> 2) * 48 + (ii - ((ii + 3 - hg) >> 2)) * 4 + wid;
    const int bq = wv_ / CPB, part = wv_ - bq * CPB;
    const int bk = bq / 6, r6 = bq - 6 * bk, k = bk < 8 ? 8 - bk : 0, fs = hg + 4 * (r6 & 1), m = r6 >> 1;
    const char* pb = m == 2 ? (const char*)w2 + (size_t)(fs * 576 + 64 * k) * 1152 : (m == 0 ? (const char*)w1 : (const char*)w3) + fs * 1152 + k * 128;
    const unsigned st = prod ? 0u : (m == 2 ? 128u : 9216u);
#pragma unroll
    for (int j = 0; j < NPF; ++j) {
      const unsigned vo = ((part * NPF + j) * 64 + lane) * st;
      asm volatile("global_load_dword %0, %1, %2" : "=v"(pf[j]) : "v"(vo), "s"(pb) : "memory");
    }
  }
  u32x4 Q0[4]; unsigned qe[4];
  const u32x4 K0 = KK[0], K1 = KK[1], K2 = KK[2], K3 = KK[3];
  const unsigned ldsb = (unsigned)(size_t)lds;
  const int rho = lane >> 3;
  const unsigned cswz = ((lane & 7) ^ (4 * ((rho >> 1) & 1))) * 16;
  const unsigned vmain = rho * 1152 + cswz;
  const int tt = wid & 1;
  const char* wob = (const char*)wo + (unsigned)__builtin_amdgcn_readfirstlane(18 * 8 * hg * 1152);
  {
    LAS char* qx = lds + (32 * tbk + tok) * 400 + 4 * hh * 48;
#pragma unroll
    for (int t = 0; t < 4; ++t) { Q0[t] = *(LAS u32x4*)(qx + t * 48 + hi * 16); qe[t] = *(LAS unsigned*)(qx + t * 48 + 32); }
  }
  const int G = lane >> 4, g1 = G & 1, qq = (lane & 15) >> 2, pp = lane & 3;
  const int swz = 4 * ((qq >> 1) & 1);
  const unsigned lb0 = (4 * hi + qq) * 128 + 8 * (pp & 1);
  LAS char* trE = lds + lb0 + (((4 * hh + 2 * g1 + (pp >> 1)) ^ swz) * 16) + hh * 4096;
  LAS char* trO = lds + lb0 + (((4 * (hh ^ 1) + 2 * g1 + (pp >> 1)) ^ swz) * 16) + hh * 5120;
#define WOFRAG(base_, b_) trread(((b_) & 1) ? trO : trE, (base_) + ((b_) >> 1) * 1024, (base_) + 9216 + ((b_) >> 1) * 1024)
  bf16x8 A7[9], A8[9];
#pragma unroll
  for (int b = 0; b < 9; ++b) { A7[b] = WOFRAG(WO_R2, b); A8[b] = WOFRAG(WO_R2 + 18432, b); }
  asm volatile("s_waitcnt lgkmcnt(0)" : "+v"(A7[0]), "+v"(A7[1]), "+v"(A7[2]), "+v"(A7[3]), "+v"(A7[4]), "+v"(A7[5]), "+v"(A7[6]), "+v"(A7[7]), "+v"(A7[8]) :: "memory");
  asm volatile("" : "+v"(A8[0]), "+v"(A8[1]), "+v"(A8[2]), "+v"(A8[3]), "+v"(A8[4]), "+v"(A8[5]), "+v"(A8[6]), "+v"(A8[7]), "+v"(A8[8]) :: "memory");
  asm volatile("" : "+v"(Q0[0]), "+v"(Q0[1]), "+v"(Q0[2]), "+v"(Q0[3]), "+v"(qe[0]), "+v"(qe[1]), "+v"(qe[2]), "+v"(qe[3]) :: "memory");
  BAR();
  {
    const unsigned lw2 = __builtin_amdgcn_readfirstlane(ldsb + wid * 9216);
    wo_row9<0>(vmain, lw2, wob + (unsigned)__builtin_amdgcn_readfirstlane((18 * hh + 8 * tt) * 1152));
  }
  {
    const unsigned lw = __builtin_amdgcn_readfirstlane(ldsb + WO_R2 + tt * 9216 + hh * 1024);
    const char* g = wob + (unsigned)__builtin_amdgcn_readfirstlane((18 * 6 + 8 * tt) * 1152 + hh * 128);
    glds2<0, 0>(vmain, lw, g); glds2<2048, 256>(vmain, lw, g); glds2<4096, 512>(vmain, lw, g); glds2<6144, 768>(vmain, lw, g);
    const unsigned lw5 = __builtin_amdgcn_readfirstlane(lw - hh * 2048);
    glds2<8192, 1024>(vmain, lw5, wob + (unsigned)__builtin_amdgcn_readfirstlane((18 * 6 + 8 * tt) * 1152 - hh * 128));
  }
  asm volatile("" : "+v"(Q0[0]), "+v"(Q0[1]), "+v"(Q0[2]), "+v"(Q0[3]), "+v"(qe[0]), "+v"(qe[1]), "+v"(qe[2]), "+v"(qe[3]) :: "memory");
  bf16x8 Qa[4], Q1[4];
  const f32x16 zero = {};
#pragma unroll
  for (int t = 0; t < 4; ++t) {
    Qa[t] = __builtin_bit_cast(bf16x8, Q0[t]);
    u32x4 x = {hi ? 0u : qe[t], 0u, 0u, 0u};
    Q1[t] = __builtin_bit_cast(bf16x8, x);
    f32x16 s = MFMA32(__builtin_bit_cast(bf16x8, K0), Qa[t], zero); s = MFMA32(__builtin_bit_cast(bf16x8, K1), Q1[t], s);
    float m = xhalf_max(max16(s)) + MSHIFT;
    unsigned mb = (unsigned)f2bf(-m);
    x[1] = hi ? 0u : mb; Q1[t] = __builtin_bit_cast(bf16x8, x);
  }
  f32x16 O[4];
#pragma unroll
  for (int t = 0; t < 4; ++t) O[t] = zero;
  {
    const unsigned va0 = lane * 16;
    asm volatile(
    "s_mov_b64 s[40:41], %[base]\n"
    "s_movk_i32 s42, 32\n"
    "global_load_dwordx4 v[200:203], %[va0], s[40:41] offset:0 sc1\n"
    "global_load_dwordx4 v[204:207], %[va0], s[40:41] offset:1024 sc1\n"
    "global_load_dwordx4 v[216:219], %[va0], s[40:41] offset:2048 sc1\n"
    "global_load_dwordx4 v[220:223], %[va0], s[40:41] offset:3072 sc1\n"
    "global_load_dwordx4 v[208:211], %[va1], s[40:41] offset:0 sc1\n"
    "global_load_dwordx4 v[212:215], %[va1], s[40:41] offset:1024 sc1\n"
    "v_mov_b32 v196, 0\n"
    "v_mov_b32 v197, 0\n"
    "v_mov_b32 v198, 0\n"
    "v_mov_b32 v199, 0\n"
    "v_mov_b32 v228, 0\n"
    "v_mov_b32 v229, 0\n"
    "v_mov_b32 v230, 0\n"
    "v_mov_b32 v231, 0\n"
    "v_mov_b32 v190, 0\n"
    "v_mov_b32 v191, 0\n"
    "s_waitcnt vmcnt(4)\n"
    "v_mfma_f32_32x32x16_bf16 v[160:175], v[200:203], %[q00], 0\n"
    "v_mfma_f32_32x32x16_bf16 v[160:175], v[204:207], %[q10], v[160:175]\n"
    "s_nop 15\n"
    "1:\n"
    "v_exp_f32 v160, v160\n"
    "v_exp_f32 v161, v161\n"
    "v_mfma_f32_32x32x16_bf16 v[176:191], v[200:203], %[q01], 0\n"
    "v_exp_f32 v162, v162\n"
    "v_exp_f32 v163, v163\n"
    "v_cvt_pk_bf16_f32 v192, v160, v161\n"
    "v_cvt_pk_bf16_f32 v193, v162, v163\n"
    "v_exp_f32 v164, v164\n"
    "v_exp_f32 v165, v165\n"
    "v_mfma_f32_32x32x16_bf16 v[176:191], v[204:207], %[q11], v[176:191]\n"
    "v_exp_f32 v166, v166\n"
    "v_exp_f32 v167, v167\n"
    "v_cvt_pk_bf16_f32 v194, v164, v165\n"
    "v_cvt_pk_bf16_f32 v195, v166, v167\n"
    "v_exp_f32 v168, v168\n"
    "v_exp_f32 v169, v169\n"
    "v_mfma_f32_32x32x16_bf16 %[o3], v[228:231], v[196:199], %[o3]\n"
    "global_load_dwordx4 v[224:227], %[va1], s[40:41] offset:2048 sc1\n"
    "global_load_dwordx4 v[228:231], %[va1], s[40:41] offset:3072 sc1\n"
    "v_exp_f32 v170, v170\n"
    "v_exp_f32 v171, v171\n"
    "v_cvt_pk_bf16_f32 v196, v168, v169\n"
    "v_cvt_pk_bf16_f32 v197, v170, v171\n"
    "v_exp_f32 v172, v172\n"
    "v_exp_f32 v173, v173\n"
    "s_waitcnt vmcnt(4)\n"
    "v_mfma_f32_32x32x16_bf16 %[o0], v[216:219], v[192:195], %[o0]\n"
    "v_exp_f32 v174, v174\n"
    "v_exp_f32 v175, v175\n"
    "v_cvt_pk_bf16_f32 v198, v172, v173\n"
    "v_cvt_pk_bf16_f32 v199, v174, v175\n"
    "v_exp_f32 v176, v176\n"
    "v_exp_f32 v177, v177\n"
    "v_mfma_f32_32x32x16_bf16 v[160:175], v[200:203], %[q02], 0\n"
    "v_exp_f32 v178, v178\n"
    "v_exp_f32 v179, v179\n"
    "v_cvt_pk_bf16_f32 v192, v176, v177\n"
    "v_cvt_pk_bf16_f32 v193, v178, v179\n"
    "v_exp_f32 v180, v180\n"
    "v_exp_f32 v181, v181\n"
    "v_mfma_f32_32x32x16_bf16 v[160:175], v[204:207], %[q12], v[160:175]\n"
    "v_exp_f32 v182, v182\n"
    "v_exp_f32 v183, v183\n"
    "v_cvt_pk_bf16_f32 v194, v180, v181\n"
    "v_cvt_pk_bf16_f32 v195, v182, v183\n"
    "v_exp_f32 v184, v184\n"
    "v_exp_f32 v185, v185\n"
    "v_mfma_f32_32x32x16_bf16 %[o0], v[220:223], v[196:199], %[o0]\n"
    "v_exp_f32 v186, v186\n"
    "v_exp_f32 v187, v187\n"
    "v_cvt_pk_bf16_f32 v196, v184, v185\n"
    "v_cvt_pk_bf16_f32 v197, v186, v187\n"
    "v_exp_f32 v188, v188\n"
    "v_exp_f32 v189, v189\n"
    "v_mfma_f32_32x32x16_bf16 %[o1], v[216:219], v[192:195], %[o1]\n"
    "v_exp_f32 v190, v190\n"
    "v_exp_f32 v191, v191\n"
    "v_cvt_pk_bf16_f32 v198, v188, v189\n"
    "v_cvt_pk_bf16_f32 v199, v190, v191\n"
    "v_exp_f32 v160, v160\n"
    "v_exp_f32 v161, v161\n"
    "v_mfma_f32_32x32x16_bf16 v[176:191], v[200:203], %[q03], 0\n"
    "v_exp_f32 v162, v162\n"
    "v_exp_f32 v163, v163\n"
    "v_cvt_pk_bf16_f32 v192, v160, v161\n"
    "v_cvt_pk_bf16_f32 v193, v162, v163\n"
    "v_exp_f32 v164, v164\n"
    "v_exp_f32 v165, v165\n"
    "v_mfma_f32_32x32x16_bf16 v[176:191], v[204:207], %[q13], v[176:191]\n"
    "global_load_dwordx4 v[200:203], %[va2], s[40:41] offset:0 sc1\n"
    "global_load_dwordx4 v[204:207], %[va2], s[40:41] offset:1024 sc1\n"
    "v_exp_f32 v166, v166\n"
    "v_exp_f32 v167, v167\n"
    "v_cvt_pk_bf16_f32 v194, v164, v165\n"
    "v_cvt_pk_bf16_f32 v195, v166, v167\n"
    "v_exp_f32 v168, v168\n"
    "v_exp_f32 v169, v169\n"
    "v_mfma_f32_32x32x16_bf16 %[o1], v[220:223], v[196:199], %[o1]\n"
    "v_exp_f32 v170, v170\n"
    "v_exp_f32 v171, v171\n"
    "v_cvt_pk_bf16_f32 v196, v168, v169\n"
    "v_cvt_pk_bf16_f32 v197, v170, v171\n"
    "v_exp_f32 v172, v172\n"
    "v_exp_f32 v173, v173\n"
    "v_mfma_f32_32x32x16_bf16 %[o2], v[216:219], v[192:195], %[o2]\n"
    "v_exp_f32 v174, v174\n"
    "v_exp_f32 v175, v175\n"
    "v_cvt_pk_bf16_f32 v198, v172, v173\n"
    "v_cvt_pk_bf16_f32 v199, v174, v175\n"
    "v_exp_f32 v176, v176\n"
    "v_exp_f32 v177, v177\n"
    "s_waitcnt vmcnt(4)\n"
    "v_mfma_f32_32x32x16_bf16 v[160:175], v[208:211], %[q00], 0\n"
    "v_exp_f32 v178, v178\n"
    "v_exp_f32 v179, v179\n"
    "v_cvt_pk_bf16_f32 v192, v176, v177\n"
    "v_cvt_pk_bf16_f32 v193, v178, v179\n"
    "v_exp_f32 v180, v180\n"
    "v_exp_f32 v181, v181\n"
    "v_mfma_f32_32x32x16_bf16 v[160:175], v[212:215], %[q10], v[160:175]\n"
    "v_exp_f32 v182, v182\n"
    "v_exp_f32 v183, v183\n"
    "v_cvt_pk_bf16_f32 v194, v180, v181\n"
    "v_cvt_pk_bf16_f32 v195, v182, v183\n"
    "v_exp_f32 v184, v184\n"
    "v_exp_f32 v185, v185\n"
    "v_mfma_f32_32x32x16_bf16 %[o2], v[220:223], v[196:199], %[o2]\n"
    "v_exp_f32 v186, v186\n"
    "v_exp_f32 v187, v187\n"
    "v_cvt_pk_bf16_f32 v196, v184, v185\n"
    "v_cvt_pk_bf16_f32 v197, v186, v187\n"
    "v_exp_f32 v188, v188\n"
    "v_exp_f32 v189, v189\n"
    "v_mfma_f32_32x32x16_bf16 %[o3], v[216:219], v[192:195], %[o3]\n"
    "v_exp_f32 v190, v190\n"
    "v_exp_f32 v191, v191\n"
    "v_cvt_pk_bf16_f32 v198, v188, v189\n"
    "v_cvt_pk_bf16_f32 v199, v190, v191\n"
    "v_exp_f32 v160, v160\n"
    "v_exp_f32 v161, v161\n"
    "v_mfma_f32_32x32x16_bf16 v[176:191], v[208:211], %[q01], 0\n"
    "v_exp_f32 v162, v162\n"
    "v_exp_f32 v163, v163\n"
    "v_cvt_pk_bf16_f32 v192, v160, v161\n"
    "v_cvt_pk_bf16_f32 v193, v162, v163\n"
    "v_exp_f32 v164, v164\n"
    "v_exp_f32 v165, v165\n"
    "v_mfma_f32_32x32x16_bf16 v[176:191], v[212:215], %[q11], v[176:191]\n"
    "v_exp_f32 v166, v166\n"
    "v_exp_f32 v167, v167\n"
    "v_cvt_pk_bf16_f32 v194, v164, v165\n"
    "v_cvt_pk_bf16_f32 v195, v166, v167\n"
    "v_exp_f32 v168, v168\n"
    "v_exp_f32 v169, v169\n"
    "v_mfma_f32_32x32x16_bf16 %[o3], v[220:223], v[196:199], %[o3]\n"
    "global_load_dwordx4 v[216:219], %[va2], s[40:41] offset:2048 sc1\n"
    "global_load_dwordx4 v[220:223], %[va2], s[40:41] offset:3072 sc1\n"
    "v_exp_f32 v170, v170\n"
    "v_exp_f32 v171, v171\n"
    "v_cvt_pk_bf16_f32 v196, v168, v169\n"
    "v_cvt_pk_bf16_f32 v197, v170, v171\n"
    "v_exp_f32 v172, v172\n"
    "v_exp_f32 v173, v173\n"
    "s_waitcnt vmcnt(4)\n"
    "v_mfma_f32_32x32x16_bf16 %[o0], v[224:227], v[192:195], %[o0]\n"
    "v_exp_f32 v174, v174\n"
    "v_exp_f32 v175, v175\n"
    "v_cvt_pk_bf16_f32 v198, v172, v173\n"
    "v_cvt_pk_bf16_f32 v199, v174, v175\n"
    "v_exp_f32 v176, v176\n"
    "v_exp_f32 v177, v177\n"
    "v_mfma_f32_32x32x16_bf16 v[160:175], v[208:211], %[q02], 0\n"
    "v_exp_f32 v178, v178\n"
    "v_exp_f32 v179, v179\n"
    "v_cvt_pk_bf16_f32 v192, v176, v177\n"
    "v_cvt_pk_bf16_f32 v193, v178, v179\n"
    "v_exp_f32 v180, v180\n"
    "v_exp_f32 v181, v181\n"
    "v_mfma_f32_32x32x16_bf16 v[160:175], v[212:215], %[q12], v[160:175]\n"
    "v_exp_f32 v182, v182\n"
    "v_exp_f32 v183, v183\n"
    "v_cvt_pk_bf16_f32 v194, v180, v181\n"
    "v_cvt_pk_bf16_f32 v195, v182, v183\n"
    "v_exp_f32 v184, v184\n"
    "v_exp_f32 v185, v185\n"
    "v_mfma_f32_32x32x16_bf16 %[o0], v[228:231], v[196:199], %[o0]\n"
    "v_exp_f32 v186, v186\n"
    "v_exp_f32 v187, v187\n"
    "v_cvt_pk_bf16_f32 v196, v184, v185\n"
    "v_cvt_pk_bf16_f32 v197, v186, v187\n"
    "v_exp_f32 v188, v188\n"
    "v_exp_f32 v189, v189\n"
    "v_mfma_f32_32x32x16_bf16 %[o1], v[224:227], v[192:195], %[o1]\n"
    "v_exp_f32 v190, v190\n"
    "v_exp_f32 v191, v191\n"
    "v_cvt_pk_bf16_f32 v198, v188, v189\n"
    "v_cvt_pk_bf16_f32 v199, v190, v191\n"
    "v_exp_f32 v160, v160\n"
    "v_exp_f32 v161, v161\n"
    "v_mfma_f32_32x32x16_bf16 v[176:191], v[208:211], %[q03], 0\n"
    "v_exp_f32 v162, v162\n"
    "v_exp_f32 v163, v163\n"
    "v_cvt_pk_bf16_f32 v192, v160, v161\n"
    "v_cvt_pk_bf16_f32 v193, v162, v163\n"
    "v_exp_f32 v164, v164\n"
    "v_exp_f32 v165, v165\n"
    "v_mfma_f32_32x32x16_bf16 v[176:191], v[212:215], %[q13], v[176:191]\n"
    "global_load_dwordx4 v[208:211], %[va3], s[40:41] offset:0 sc1\n"
    "global_load_dwordx4 v[212:215], %[va3], s[40:41] offset:1024 sc1\n"
    "v_exp_f32 v166, v166\n"
    "v_exp_f32 v167, v167\n"
    "v_cvt_pk_bf16_f32 v194, v164, v165\n"
    "v_cvt_pk_bf16_f32 v195, v166, v167\n"
    "v_exp_f32 v168, v168\n"
    "v_exp_f32 v169, v169\n"
    "v_mfma_f32_32x32x16_bf16 %[o1], v[228:231], v[196:199], %[o1]\n"
    "v_exp_f32 v170, v170\n"
    "v_exp_f32 v171, v171\n"
    "v_cvt_pk_bf16_f32 v196, v168, v169\n"
    "v_cvt_pk_bf16_f32 v197, v170, v171\n"
    "v_exp_f32 v172, v172\n"
    "v_exp_f32 v173, v173\n"
    "v_mfma_f32_32x32x16_bf16 %[o2], v[224:227], v[192:195], %[o2]\n"
    "v_exp_f32 v174, v174\n"
    "v_exp_f32 v175, v175\n"
    "v_cvt_pk_bf16_f32 v198, v172, v173\n"
    "v_cvt_pk_bf16_f32 v199, v174, v175\n"
    "v_exp_f32 v176, v176\n"
    "v_exp_f32 v177, v177\n"
    "s_waitcnt vmcnt(4)\n"
    "v_mfma_f32_32x32x16_bf16 v[160:175], v[200:203], %[q00], 0\n"
    "v_exp_f32 v178, v178\n"
    "v_exp_f32 v179, v179\n"
    "v_cvt_pk_bf16_f32 v192, v176, v177\n"
    "v_cvt_pk_bf16_f32 v193, v178, v179\n"
    "v_exp_f32 v180, v180\n"
    "v_exp_f32 v181, v181\n"
    "v_mfma_f32_32x32x16_bf16 v[160:175], v[204:207], %[q10], v[160:175]\n"
    "v_exp_f32 v182, v182\n"
    "v_exp_f32 v183, v183\n"
    "v_cvt_pk_bf16_f32 v194, v180, v181\n"
    "v_cvt_pk_bf16_f32 v195, v182, v183\n"
    "v_exp_f32 v184, v184\n"
    "v_exp_f32 v185, v185\n"
    "v_mfma_f32_32x32x16_bf16 %[o2], v[228:231], v[196:199], %[o2]\n"
    "v_exp_f32 v186, v186\n"
    "v_exp_f32 v187, v187\n"
    "v_cvt_pk_bf16_f32 v196, v184, v185\n"
    "v_cvt_pk_bf16_f32 v197, v186, v187\n"
    "v_exp_f32 v188, v188\n"
    "v_exp_f32 v189, v189\n"
    "v_mfma_f32_32x32x16_bf16 %[o3], v[224:227], v[192:195], %[o3]\n"
    "v_exp_f32 v190, v190\n"
    "v_exp_f32 v191, v191\n"
    "v_cvt_pk_bf16_f32 v198, v188, v189\n"
    "v_cvt_pk_bf16_f32 v199, v190, v191\n"
    "s_add_u32 s40, s40, 8192\n"
    "s_addc_u32 s41, s41, 0\n"
    "s_sub_u32 s42, s42, 1\n"
    "s_cmp_lg_u32 s42, 0\n"
    "s_cbranch_scc1 1b\n"
    "s_nop 1\n"
    "v_mfma_f32_32x32x16_bf16 %[o3], v[228:231], v[196:199], %[o3]\n"
    "s_waitcnt vmcnt(0)\n"
    "s_nop 15\n"
      : [o0] "+v"(O[0]), [o1] "+v"(O[1]), [o2] "+v"(O[2]), [o3] "+v"(O[3])
      : [q00] "v"(Qa[0]), [q01] "v"(Qa[1]), [q02] "v"(Qa[2]), [q03] "v"(Qa[3]), [q10] "v"(Q1[0]), [q11] "v"(Q1[1]), [q12] "v"(Q1[2]), [q13] "v"(Q1[3]),
        [va0] "v"(va0), [va1] "v"(va0 + 4096u), [va2] "v"(va0 + 8192u), [va3] "v"(va0 + 12288u), [base] "s"(kvf)
      : "memory", "scc", "s40", "s41", "s42", V10(16), V10(17), V10(18), V10(19), V10(20), V10(21), V10(22), "v230", "v231");
  }
#pragma unroll
  for (int j = 0; j < NPF; ++j) asm volatile("" : "+v"(pf[j]));
  bool bad = false;
#pragma unroll
  for (int t = 0; t < 4; ++t) {
    float l = hi ? O[t][8] : O[t][10];
    bad |= !(l < 4.0e37f);
#pragma unroll
    for (int r = 0; r < 10; ++r) bad |= !(fabsf(O[t][r]) < 3.0e38f);
  }
  if (__any(bad)) {
    __builtin_amdgcn_fence(__ATOMIC_ACQUIRE, "agent");
    const bf16x8* kv = (const bf16x8*)kvf + lane;
#pragma unroll
    for (int t = 0; t < 4; ++t) {
      u32x4 x = __builtin_bit_cast(u32x4, Q1[t]); x[1] = 0u; bf16x8 q1 = __builtin_bit_cast(bf16x8, x);
      f32x16 acc = zero; float m = -3.0e38f;
      for (int it = 0; it < 64; ++it) {
        bf16x8 k0 = kv[it * 256], k1 = kv[it * 256 + 64], v0 = kv[it * 256 + 128], v1 = kv[it * 256 + 192];
        f32x16 s = MFMA32(k0, Qa[t], zero); s = MFMA32(k1, q1, s);
        float mn = fmaxf(m, xhalf_max(max16(s)));
        float a = __builtin_amdgcn_exp2f(m - mn); m = mn;
#pragma unroll
        for (int r = 0; r < 16; ++r) { acc[r] *= a; s[r] -= mn; }
        bf16x8 p0, p1; exp_pack(s, p0, p1);
        acc = MFMA32(v0, p0, acc); acc = MFMA32(v1, p1, acc);
      }
      O[t] = acc;
    }
  }
  LAS char* ex = lds + WO_R2 + 18432 + tbk * 9216;
  {
    u32x4 e;
#pragma unroll
    for (int t = 0; t < 4; ++t) {
      const float rl = __builtin_amdgcn_rcpf(hi ? O[t][8] : O[t][10]);
      u32x4 o = {cvtpk(O[t][0] * rl, O[t][1] * rl), cvtpk(O[t][2] * rl, O[t][3] * rl), cvtpk(O[t][4] * rl, O[t][5] * rl), cvtpk(O[t][6] * rl, O[t][7] * rl)};
      *(LAS u32x4*)(ex + (4 * hh + t) * 1024 + lane * 16) = o;
      e[t] = cvtpk(O[t][8] * rl, O[t][9] * rl);
    }
    if (!hi) *(LAS u32x4*)(ex + 8 * 1024 + (hh * 32 + tok) * 16) = e;
  }
  asm volatile("s_waitcnt vmcnt(0) lgkmcnt(0)" ::: "memory");
  __builtin_amdgcn_s_barrier();
  SCHED();
  bf16x8 B[9];
#pragma unroll
  for (int ks = 0; ks < 9; ++ks) B[ks] = *(LAS bf16x8*)(ex + ks * 1024 + lane * 16);
  f32x16 acc[9];
#pragma unroll
  for (int b = 0; b < 9; ++b) { acc[b] = MFMA32(A7[b], B[7], zero); acc[b] = MFMA32(A8[b], B[8], acc[b]); }
#pragma unroll
  for (int ks = 0; ks < 7; ++ks) {
#pragma unroll
    for (int b = 0; b < 9; ++b) acc[b] = MFMA32(WOFRAG(ks < 6 ? ks * 18432 : WO_R2, b), B[ks], acc[b]);
  }
#undef WOFRAG
  char* dst = (char*)P + ((size_t)((8 + hg) * 64 + tb) * 36 + 18 * hh) * 1024 + lane * 16;
#pragma unroll
  for (int b = 0; b < 9; ++b) {
    u32x4 o0 = {cvtpk(acc[b][0], acc[b][1]), cvtpk(acc[b][2], acc[b][3]), cvtpk(acc[b][4], acc[b][5]), cvtpk(acc[b][6], acc[b][7])};
    u32x4 o1 = {cvtpk(acc[b][8], acc[b][9]), cvtpk(acc[b][10], acc[b][11]), cvtpk(acc[b][12], acc[b][13]), cvtpk(acc[b][14], acc[b][15])};
    *(u32x4*)(dst + (2 * b) * 1024) = o0;
    *(u32x4*)(dst + (2 * b + 1) * 1024) = o1;
  }
  if (ab == 0 && wid == 0) {
    unsigned spins = 0;
    while (true) {
      const unsigned long long av0 = __hip_atomic_load((gu64*)(fl + 256 + lane * 8), __ATOMIC_RELAXED, __HIP_MEMORY_SCOPE_AGENT);
      const unsigned long long av1 = __hip_atomic_load((gu64*)(fl + 768 + lane * 8), __ATOMIC_RELAXED, __HIP_MEMORY_SCOPE_AGENT);
      if (__all(av0 == tag && av1 == tag) || ++spins > SPINCAP) break;
      __builtin_amdgcn_s_sleep(2);
    }
    if (lane == 0) __hip_atomic_store((gu32*)fl, ep + 1u, __ATOMIC_RELAXED, __HIP_MEMORY_SCOPE_AGENT);
  }
}

constexpr int FFN_LDS = 156672;
DI void ffn_body(unsigned lds, const u16* xnp, const u16* w1, const u16* w3, const u16* w2, u16* P, int tb, int fs, int wid) {
  const char* xp = (const char*)xnp + (size_t)tb * 36864;
  char* pp = (char*)P + (size_t)(fs * 64 + tb) * 36864;
  asm volatile(
"v_mbcnt_lo_u32_b32 v192, -1, 0\n"
"v_mbcnt_hi_u32_b32 v192, -1, v192\n"
"s_mov_b64 s[82:83], %[xp]\n"
"v_lshlrev_b32 v193, 4, v192\n"
"s_mov_b64 s[40:41], %[w1]\n"
"s_mov_b32 s42, 5308416\n"
"s_mov_b32 s43, 0x20000\n"
"s_mov_b64 s[44:45], %[w3]\n"
"s_mov_b32 s46, 5308416\n"
"s_mov_b32 s47, 0x20000\n"
"s_mov_b64 s[48:49], %[w2]\n"
"s_mov_b32 s50, 5308416\n"
"s_mov_b32 s51, 0x20000\n"
"s_mov_b32 s56, 294912\n"
"s_mov_b32 s57, 589824\n"
"s_mov_b32 s58, 884736\n"
"s_mov_b32 s59, 1179648\n"
"s_mov_b32 s60, 1474560\n"
"s_mov_b32 s61, 1769472\n"
"s_mov_b32 s62, 2064384\n"
"s_mov_b32 s63, 2359296\n"
"s_mov_b32 s64, 2654208\n"
"s_mov_b32 s65, 2949120\n"
"s_mov_b32 s66, 3244032\n"
"s_mov_b32 s67, 3538944\n"
"s_mov_b32 s68, 3833856\n"
"s_mov_b32 s69, 4128768\n"
"s_mov_b32 s70, 4423680\n"
"s_mov_b32 s71, 4718592\n"
"s_mov_b32 s72, 5013504\n"
"s_mul_i32 s92, %[wid], 1088\n"
"s_add_u32 s52, s92, %[lds]\n"
"s_add_u32 s53, s52, 39168\n"
"s_add_u32 s54, s52, 78336\n"
"s_add_u32 s55, s52, 117504\n"
"s_mov_b32 s74, 9\n"
"s_mov_b32 s77, 36864\n"
"s_mov_b32 s78, 0xffff0000\n"
"v_lshrrev_b32 v194, 5, v192\n"
"v_lshlrev_b32 v194, 4, v194\n"
"v_and_b32 v195, 8, v192\n"
"v_add_u32 v194, v194, v195\n"
"v_and_b32 v195, 16, v192\n"
"v_lshrrev_b32 v195, 2, v195\n"
"v_add_u32 v194, v194, v195\n"
"v_mul_u32_u24 v194, 9216, v194\n"
"v_and_b32 v195, 7, v192\n"
"v_lshlrev_b32 v195, 4, v195\n"
"s_mul_i32 s93, %[wid], 9216\n"
"s_mul_i32 s94, %[fs], 1152\n"
"s_add_u32 s93, s93, s94\n"
"v_add3_u32 v222, v194, v195, s93\n"
"v_lshrrev_b32 v194, 5, v192\n"
"v_lshlrev_b32 v194, 4, v194\n"
"v_and_b32 v223, 8, v192\n"
"v_add_u32 v194, v194, v223\n"
"v_and_b32 v223, 16, v192\n"
"v_lshrrev_b32 v223, 2, v223\n"
"v_add_u32 v194, v194, v223\n"
"v_mul_u32_u24 v194, 1152, v194\n"
"s_mul_i32 s93, %[wid], 1152\n"
"s_mul_i32 s94, %[fs], 663552\n"
"s_add_u32 s93, s93, s94\n"
"v_add3_u32 v223, v194, v195, s93\n"
"v_bfe_u32 v194, v192, 2, 2\n"
"v_mul_u32_u24 v194, 1088, v194\n"
"v_lshrrev_b32 v195, 5, v192\n"
"v_lshlrev_b32 v195, 8, v195\n"
"v_add_u32 v194, v194, v195\n"
"v_and_b32 v195, 16, v192\n"
"v_lshlrev_b32 v195, 1, v195\n"
"v_add_u32 v194, v194, v195\n"
"v_and_b32 v195, 3, v192\n"
"v_lshlrev_b32 v195, 3, v195\n"
"v_add_u32 v194, v194, v195\n"
"v_subrev_u32 v221, s92, v194\n"
"s_mul_i32 s75, %[wid], 36864\n"
"s_add_u32 s75, s75, %[lds]\n"
"v_lshrrev_b32 v248, 3, v192\n"
"v_lshrrev_b32 v249, 4, v192\n"
"v_and_b32 v250, 7, v192\n"
"v_xor_b32 v249, v249, v250\n"
"v_lshlrev_b32 v249, 4, v249\n"
"v_mul_u32_u24 v248, 1152, v248\n"
"v_add_u32 v236, v248, v249\n"
"v_xor_b32 v249, 64, v249\n"
"v_add_u32 v237, v248, v249\n"
"v_add_u32 v237, 9216, v237\n"
"v_add_u32 v238, 18432, v236\n"
"v_add_u32 v239, 18432, v237\n"
"s_add_u32 m0, s75, 0\n"
"s_nop 0\n"
"global_load_lds_dwordx4 v236, s[82:83] offset:0\n"
"s_add_u32 m0, s75, 9216\n"
"s_nop 0\n"
"global_load_lds_dwordx4 v237, s[82:83] offset:0\n"
"s_add_u32 m0, s75, 18432\n"
"s_nop 0\n"
"global_load_lds_dwordx4 v238, s[82:83] offset:0\n"
"s_add_u32 m0, s75, 27648\n"
"s_nop 0\n"
"global_load_lds_dwordx4 v239, s[82:83] offset:0\n"
"s_add_u32 m0, s75, 896\n"
"s_nop 0\n"
"global_load_lds_dwordx4 v236, s[82:83] offset:128\n"
"s_add_u32 m0, s75, 10112\n"
"s_nop 0\n"
"global_load_lds_dwordx4 v237, s[82:83] offset:128\n"
"s_add_u32 m0, s75, 19328\n"
"s_nop 0\n"
"global_load_lds_dwordx4 v238, s[82:83] offset:128\n"
"s_add_u32 m0, s75, 28544\n"
"s_nop 0\n"
"global_load_lds_dwordx4 v239, s[82:83] offset:128\n"
"s_add_u32 m0, s75, 1792\n"
"s_nop 0\n"
"global_load_lds_dwordx4 v236, s[82:83] offset:256\n"
"s_add_u32 m0, s75, 11008\n"
"s_nop 0\n"
"global_load_lds_dwordx4 v237, s[82:83] offset:256\n"
"s_add_u32 m0, s75, 20224\n"
"s_nop 0\n"
"global_load_lds_dwordx4 v238, s[82:83] offset:256\n"
"s_add_u32 m0, s75, 29440\n"
"s_nop 0\n"
"global_load_lds_dwordx4 v239, s[82:83] offset:256\n"
"s_add_u32 m0, s75, 2688\n"
"s_nop 0\n"
"global_load_lds_dwordx4 v236, s[82:83] offset:384\n"
"s_add_u32 m0, s75, 11904\n"
"s_nop 0\n"
"global_load_lds_dwordx4 v237, s[82:83] offset:384\n"
"s_add_u32 m0, s75, 21120\n"
"s_nop 0\n"
"global_load_lds_dwordx4 v238, s[82:83] offset:384\n"
"s_add_u32 m0, s75, 30336\n"
"s_nop 0\n"
"global_load_lds_dwordx4 v239, s[82:83] offset:384\n"
"s_add_u32 m0, s75, 3584\n"
"s_nop 0\n"
"global_load_lds_dwordx4 v236, s[82:83] offset:512\n"
"s_add_u32 m0, s75, 12800\n"
"s_nop 0\n"
"global_load_lds_dwordx4 v237, s[82:83] offset:512\n"
"s_add_u32 m0, s75, 22016\n"
"s_nop 0\n"
"global_load_lds_dwordx4 v238, s[82:83] offset:512\n"
"s_add_u32 m0, s75, 31232\n"
"s_nop 0\n"
"global_load_lds_dwordx4 v239, s[82:83] offset:512\n"
"s_add_u32 m0, s75, 4480\n"
"s_nop 0\n"
"global_load_lds_dwordx4 v236, s[82:83] offset:640\n"
"s_add_u32 m0, s75, 13696\n"
"s_nop 0\n"
"global_load_lds_dwordx4 v237, s[82:83] offset:640\n"
"s_add_u32 m0, s75, 22912\n"
"s_nop 0\n"
"global_load_lds_dwordx4 v238, s[82:83] offset:640\n"
"s_add_u32 m0, s75, 32128\n"
"s_nop 0\n"
"global_load_lds_dwordx4 v239, s[82:83] offset:640\n"
"s_add_u32 m0, s75, 5376\n"
"s_nop 0\n"
"global_load_lds_dwordx4 v236, s[82:83] offset:768\n"
"s_add_u32 m0, s75, 14592\n"
"s_nop 0\n"
"global_load_lds_dwordx4 v237, s[82:83] offset:768\n"
"s_add_u32 m0, s75, 23808\n"
"s_nop 0\n"
"global_load_lds_dwordx4 v238, s[82:83] offset:768\n"
"s_add_u32 m0, s75, 33024\n"
"s_nop 0\n"
"global_load_lds_dwordx4 v239, s[82:83] offset:768\n"
"s_add_u32 m0, s75, 6272\n"
"s_nop 0\n"
"global_load_lds_dwordx4 v236, s[82:83] offset:896\n"
"s_add_u32 m0, s75, 15488\n"
"s_nop 0\n"
"global_load_lds_dwordx4 v237, s[82:83] offset:896\n"
"s_add_u32 m0, s75, 24704\n"
"s_nop 0\n"
"global_load_lds_dwordx4 v238, s[82:83] offset:896\n"
"s_add_u32 m0, s75, 33920\n"
"s_nop 0\n"
"global_load_lds_dwordx4 v239, s[82:83] offset:896\n"
"s_add_u32 m0, s75, 7168\n"
"s_nop 0\n"
"global_load_lds_dwordx4 v236, s[82:83] offset:1024\n"
"s_add_u32 m0, s75, 16384\n"
"s_nop 0\n"
"global_load_lds_dwordx4 v237, s[82:83] offset:1024\n"
"s_add_u32 m0, s75, 25600\n"
"s_nop 0\n"
"global_load_lds_dwordx4 v238, s[82:83] offset:1024\n"
"s_add_u32 m0, s75, 34816\n"
"s_nop 0\n"
"global_load_lds_dwordx4 v239, s[82:83] offset:1024\n"
"v_and_b32 v248, 7, v192\n"
"v_mul_u32_u24 v248, 36864, v248\n"
"v_lshrrev_b32 v249, 3, v192\n"
"v_mul_u32_u24 v249, 294912, v249\n"
"s_mul_i32 s93, %[wid], 9216\n"
"s_mul_i32 s94, %[fs], 1152\n"
"s_add_u32 s93, s93, s94\n"
"v_add3_u32 v248, v248, v249, s93\n"
"buffer_load_dword v244, v248, s[40:43], 0 offen\n"
"buffer_load_dword v245, v248, s[40:43], s63 offen\n"
"v_and_b32 v250, 7, v192\n"
"v_mul_u32_u24 v250, 36864, v250\n"
"v_bfe_u32 v249, v192, 3, 1\n"
"v_mul_u32_u24 v249, 294912, v249\n"
"v_add3_u32 v250, v250, v249, s93\n"
"buffer_load_dword v246, v250, s[40:43], s71 offen\n"
"v_and_b32 v248, 31, v192\n"
"v_lshrrev_b32 v249, 3, v248\n"
"v_mul_u32_u24 v249, 9216, v249\n"
"v_and_b32 v250, 7, v248\n"
"v_lshlrev_b32 v250, 7, v250\n"
"v_add3_u32 v249, v249, v250, s75\n"
"v_bfe_u32 v250, v248, 1, 3\n"
"v_lshrrev_b32 v251, 5, v192\n"
"v_xor_b32 v250, v250, v251\n"
"v_lshlrev_b32 v250, 4, v250\n"
"v_add_u32 v240, v249, v250\n"
"v_xor_b32 v241, 32, v240\n"
"v_xor_b32 v242, 64, v240\n"
"v_xor_b32 v243, 96, v240\n"
"v_mov_b32 v224, 0x3f803f80\n"
"v_mov_b32 v225, 0x3f803f80\n"
"v_mov_b32 v226, 0x3f803f80\n"
"v_mov_b32 v227, 0x3f803f80\n"
"v_lshlrev_b32 v251, 3, v251\n"
"v_sub_u32 v251, v248, v251\n"
"v_mov_b32 v252, 0x3f80\n"
"v_mov_b32 v253, 0x3f800000\n"
"v_cmp_eq_u32 vcc, 0, v251\n"
"s_nop 1\n"
"v_cndmask_b32 v228, 0, v252, vcc\n"
"v_cmp_eq_u32 vcc, 1, v251\n"
"s_nop 1\n"
"v_cndmask_b32 v228, v228, v253, vcc\n"
"v_cmp_eq_u32 vcc, 2, v251\n"
"s_nop 1\n"
"v_cndmask_b32 v229, 0, v252, vcc\n"
"v_cmp_eq_u32 vcc, 3, v251\n"
"s_nop 1\n"
"v_cndmask_b32 v229, v229, v253, vcc\n"
"v_cmp_eq_u32 vcc, 4, v251\n"
"s_nop 1\n"
"v_cndmask_b32 v230, 0, v252, vcc\n"
"v_cmp_eq_u32 vcc, 5, v251\n"
"s_nop 1\n"
"v_cndmask_b32 v230, v230, v253, vcc\n"
"v_cmp_eq_u32 vcc, 6, v251\n"
"s_nop 1\n"
"v_cndmask_b32 v231, 0, v252, vcc\n"
"v_cmp_eq_u32 vcc, 7, v251\n"
"s_nop 1\n"
"v_cndmask_b32 v231, v231, v253, vcc\n"
"v_cmp_eq_u32 vcc, 16, v251\n"
"s_nop 1\n"
"v_cndmask_b32 v232, 0, v252, vcc\n"
"v_cmp_eq_u32 vcc, 17, v251\n"
"s_nop 1\n"
"v_cndmask_b32 v232, v232, v253, vcc\n"
"v_cmp_eq_u32 vcc, 18, v251\n"
"s_nop 1\n"
"v_cndmask_b32 v233, 0, v252, vcc\n"
"v_cmp_eq_u32 vcc, 19, v251\n"
"s_nop 1\n"
"v_cndmask_b32 v233, v233, v253, vcc\n"
"v_cmp_eq_u32 vcc, 20, v251\n"
"s_nop 1\n"
"v_cndmask_b32 v234, 0, v252, vcc\n"
"v_cmp_eq_u32 vcc, 21, v251\n"
"s_nop 1\n"
"v_cndmask_b32 v234, v234, v253, vcc\n"
"v_cmp_eq_u32 vcc, 22, v251\n"
"s_nop 1\n"
"v_cndmask_b32 v235, 0, v252, vcc\n"
"v_cmp_eq_u32 vcc, 23, v251\n"
"s_nop 1\n"
"v_cndmask_b32 v235, v235, v253, vcc\n"
"v_accvgpr_write_b32 a0, 0\n"
"v_accvgpr_write_b32 a1, 0\n"
"v_accvgpr_write_b32 a2, 0\n"
"v_accvgpr_write_b32 a3, 0\n"
"v_accvgpr_write_b32 a4, 0\n"
"v_accvgpr_write_b32 a5, 0\n"
"v_accvgpr_write_b32 a6, 0\n"
"v_accvgpr_write_b32 a7, 0\n"
"v_accvgpr_write_b32 a8, 0\n"
"v_accvgpr_write_b32 a9, 0\n"
"v_accvgpr_write_b32 a10, 0\n"
"v_accvgpr_write_b32 a11, 0\n"
"v_accvgpr_write_b32 a12, 0\n"
"v_accvgpr_write_b32 a13, 0\n"
"v_accvgpr_write_b32 a14, 0\n"
"v_accvgpr_write_b32 a15, 0\n"
"v_accvgpr_write_b32 a16, 0\n"
"v_accvgpr_write_b32 a17, 0\n"
"v_accvgpr_write_b32 a18, 0\n"
"v_accvgpr_write_b32 a19, 0\n"
"v_accvgpr_write_b32 a20, 0\n"
"v_accvgpr_write_b32 a21, 0\n"
"v_accvgpr_write_b32 a22, 0\n"
"v_accvgpr_write_b32 a23, 0\n"
"v_accvgpr_write_b32 a24, 0\n"
"v_accvgpr_write_b32 a25, 0\n"
"v_accvgpr_write_b32 a26, 0\n"
"v_accvgpr_write_b32 a27, 0\n"
"v_accvgpr_write_b32 a28, 0\n"
"v_accvgpr_write_b32 a29, 0\n"
"v_accvgpr_write_b32 a30, 0\n"
"v_accvgpr_write_b32 a31, 0\n"
"v_accvgpr_write_b32 a32, 0\n"
"v_accvgpr_write_b32 a33, 0\n"
"v_accvgpr_write_b32 a34, 0\n"
"v_accvgpr_write_b32 a35, 0\n"
"v_accvgpr_write_b32 a36, 0\n"
"v_accvgpr_write_b32 a37, 0\n"
"v_accvgpr_write_b32 a38, 0\n"
"v_accvgpr_write_b32 a39, 0\n"
"v_accvgpr_write_b32 a40, 0\n"
"v_accvgpr_write_b32 a41, 0\n"
"v_accvgpr_write_b32 a42, 0\n"
"v_accvgpr_write_b32 a43, 0\n"
"v_accvgpr_write_b32 a44, 0\n"
"v_accvgpr_write_b32 a45, 0\n"
"v_accvgpr_write_b32 a46, 0\n"
"v_accvgpr_write_b32 a47, 0\n"
"v_accvgpr_write_b32 a48, 0\n"
"v_accvgpr_write_b32 a49, 0\n"
"v_accvgpr_write_b32 a50, 0\n"
"v_accvgpr_write_b32 a51, 0\n"
"v_accvgpr_write_b32 a52, 0\n"
"v_accvgpr_write_b32 a53, 0\n"
"v_accvgpr_write_b32 a54, 0\n"
"v_accvgpr_write_b32 a55, 0\n"
"v_accvgpr_write_b32 a56, 0\n"
"v_accvgpr_write_b32 a57, 0\n"
"v_accvgpr_write_b32 a58, 0\n"
"v_accvgpr_write_b32 a59, 0\n"
"v_accvgpr_write_b32 a60, 0\n"
"v_accvgpr_write_b32 a61, 0\n"
"v_accvgpr_write_b32 a62, 0\n"
"v_accvgpr_write_b32 a63, 0\n"
"v_accvgpr_write_b32 a64, 0\n"
"v_accvgpr_write_b32 a65, 0\n"
"v_accvgpr_write_b32 a66, 0\n"
"v_accvgpr_write_b32 a67, 0\n"
"v_accvgpr_write_b32 a68, 0\n"
"v_accvgpr_write_b32 a69, 0\n"
"v_accvgpr_write_b32 a70, 0\n"
"v_accvgpr_write_b32 a71, 0\n"
"v_accvgpr_write_b32 a72, 0\n"
"v_accvgpr_write_b32 a73, 0\n"
"v_accvgpr_write_b32 a74, 0\n"
"v_accvgpr_write_b32 a75, 0\n"
"v_accvgpr_write_b32 a76, 0\n"
"v_accvgpr_write_b32 a77, 0\n"
"v_accvgpr_write_b32 a78, 0\n"
"v_accvgpr_write_b32 a79, 0\n"
"v_accvgpr_write_b32 a80, 0\n"
"v_accvgpr_write_b32 a81, 0\n"
"v_accvgpr_write_b32 a82, 0\n"
"v_accvgpr_write_b32 a83, 0\n"
"v_accvgpr_write_b32 a84, 0\n"
"v_accvgpr_write_b32 a85, 0\n"
"v_accvgpr_write_b32 a86, 0\n"
"v_accvgpr_write_b32 a87, 0\n"
"v_accvgpr_write_b32 a88, 0\n"
"v_accvgpr_write_b32 a89, 0\n"
"v_accvgpr_write_b32 a90, 0\n"
"v_accvgpr_write_b32 a91, 0\n"
"v_accvgpr_write_b32 a92, 0\n"
"v_accvgpr_write_b32 a93, 0\n"
"v_accvgpr_write_b32 a94, 0\n"
"v_accvgpr_write_b32 a95, 0\n"
"v_accvgpr_write_b32 a96, 0\n"
"v_accvgpr_write_b32 a97, 0\n"
"v_accvgpr_write_b32 a98, 0\n"
"v_accvgpr_write_b32 a99, 0\n"
"v_accvgpr_write_b32 a100, 0\n"
"v_accvgpr_write_b32 a101, 0\n"
"v_accvgpr_write_b32 a102, 0\n"
"v_accvgpr_write_b32 a103, 0\n"
"v_accvgpr_write_b32 a104, 0\n"
"v_accvgpr_write_b32 a105, 0\n"
"v_accvgpr_write_b32 a106, 0\n"
"v_accvgpr_write_b32 a107, 0\n"
"v_accvgpr_write_b32 a108, 0\n"
"v_accvgpr_write_b32 a109, 0\n"
"v_accvgpr_write_b32 a110, 0\n"
"v_accvgpr_write_b32 a111, 0\n"
"v_accvgpr_write_b32 a112, 0\n"
"v_accvgpr_write_b32 a113, 0\n"
"v_accvgpr_write_b32 a114, 0\n"
"v_accvgpr_write_b32 a115, 0\n"
"v_accvgpr_write_b32 a116, 0\n"
"v_accvgpr_write_b32 a117, 0\n"
"v_accvgpr_write_b32 a118, 0\n"
"v_accvgpr_write_b32 a119, 0\n"
"v_accvgpr_write_b32 a120, 0\n"
"v_accvgpr_write_b32 a121, 0\n"
"v_accvgpr_write_b32 a122, 0\n"
"v_accvgpr_write_b32 a123, 0\n"
"v_accvgpr_write_b32 a124, 0\n"
"v_accvgpr_write_b32 a125, 0\n"
"v_accvgpr_write_b32 a126, 0\n"
"v_accvgpr_write_b32 a127, 0\n"
"v_accvgpr_write_b32 a128, 0\n"
"v_accvgpr_write_b32 a129, 0\n"
"v_accvgpr_write_b32 a130, 0\n"
"v_accvgpr_write_b32 a131, 0\n"
"v_accvgpr_write_b32 a132, 0\n"
"v_accvgpr_write_b32 a133, 0\n"
"v_accvgpr_write_b32 a134, 0\n"
"v_accvgpr_write_b32 a135, 0\n"
"v_accvgpr_write_b32 a136, 0\n"
"v_accvgpr_write_b32 a137, 0\n"
"v_accvgpr_write_b32 a138, 0\n"
"v_accvgpr_write_b32 a139, 0\n"
"v_accvgpr_write_b32 a140, 0\n"
"v_accvgpr_write_b32 a141, 0\n"
"v_accvgpr_write_b32 a142, 0\n"
"v_accvgpr_write_b32 a143, 0\n"
"v_accvgpr_write_b32 a144, 0\n"
"v_accvgpr_write_b32 a145, 0\n"
"v_accvgpr_write_b32 a146, 0\n"
"v_accvgpr_write_b32 a147, 0\n"
"v_accvgpr_write_b32 a148, 0\n"
"v_accvgpr_write_b32 a149, 0\n"
"v_accvgpr_write_b32 a150, 0\n"
"v_accvgpr_write_b32 a151, 0\n"
"v_accvgpr_write_b32 a152, 0\n"
"v_accvgpr_write_b32 a153, 0\n"
"v_accvgpr_write_b32 a154, 0\n"
"v_accvgpr_write_b32 a155, 0\n"
"v_accvgpr_write_b32 a156, 0\n"
"v_accvgpr_write_b32 a157, 0\n"
"v_accvgpr_write_b32 a158, 0\n"
"v_accvgpr_write_b32 a159, 0\n"
"v_accvgpr_write_b32 a160, 0\n"
"v_accvgpr_write_b32 a161, 0\n"
"v_accvgpr_write_b32 a162, 0\n"
"v_accvgpr_write_b32 a163, 0\n"
"v_accvgpr_write_b32 a164, 0\n"
"v_accvgpr_write_b32 a165, 0\n"
"v_accvgpr_write_b32 a166, 0\n"
"v_accvgpr_write_b32 a167, 0\n"
"v_accvgpr_write_b32 a168, 0\n"
"v_accvgpr_write_b32 a169, 0\n"
"v_accvgpr_write_b32 a170, 0\n"
"v_accvgpr_write_b32 a171, 0\n"
"v_accvgpr_write_b32 a172, 0\n"
"v_accvgpr_write_b32 a173, 0\n"
"v_accvgpr_write_b32 a174, 0\n"
"v_accvgpr_write_b32 a175, 0\n"
"v_accvgpr_write_b32 a176, 0\n"
"v_accvgpr_write_b32 a177, 0\n"
"v_accvgpr_write_b32 a178, 0\n"
"v_accvgpr_write_b32 a179, 0\n"
"v_accvgpr_write_b32 a180, 0\n"
"v_accvgpr_write_b32 a181, 0\n"
"v_accvgpr_write_b32 a182, 0\n"
"v_accvgpr_write_b32 a183, 0\n"
"v_accvgpr_write_b32 a184, 0\n"
"v_accvgpr_write_b32 a185, 0\n"
"v_accvgpr_write_b32 a186, 0\n"
"v_accvgpr_write_b32 a187, 0\n"
"v_accvgpr_write_b32 a188, 0\n"
"v_accvgpr_write_b32 a189, 0\n"
"v_accvgpr_write_b32 a190, 0\n"
"v_accvgpr_write_b32 a191, 0\n"
"v_accvgpr_write_b32 a192, 0\n"
"v_accvgpr_write_b32 a193, 0\n"
"v_accvgpr_write_b32 a194, 0\n"
"v_accvgpr_write_b32 a195, 0\n"
"v_accvgpr_write_b32 a196, 0\n"
"v_accvgpr_write_b32 a197, 0\n"
"v_accvgpr_write_b32 a198, 0\n"
"v_accvgpr_write_b32 a199, 0\n"
"v_accvgpr_write_b32 a200, 0\n"
"v_accvgpr_write_b32 a201, 0\n"
"v_accvgpr_write_b32 a202, 0\n"
"v_accvgpr_write_b32 a203, 0\n"
"v_accvgpr_write_b32 a204, 0\n"
"v_accvgpr_write_b32 a205, 0\n"
"v_accvgpr_write_b32 a206, 0\n"
"v_accvgpr_write_b32 a207, 0\n"
"v_accvgpr_write_b32 a208, 0\n"
"v_accvgpr_write_b32 a209, 0\n"
"v_accvgpr_write_b32 a210, 0\n"
"v_accvgpr_write_b32 a211, 0\n"
"v_accvgpr_write_b32 a212, 0\n"
"v_accvgpr_write_b32 a213, 0\n"
"v_accvgpr_write_b32 a214, 0\n"
"v_accvgpr_write_b32 a215, 0\n"
"v_accvgpr_write_b32 a216, 0\n"
"v_accvgpr_write_b32 a217, 0\n"
"v_accvgpr_write_b32 a218, 0\n"
"v_accvgpr_write_b32 a219, 0\n"
"v_accvgpr_write_b32 a220, 0\n"
"v_accvgpr_write_b32 a221, 0\n"
"v_accvgpr_write_b32 a222, 0\n"
"v_accvgpr_write_b32 a223, 0\n"
"v_accvgpr_write_b32 a224, 0\n"
"v_accvgpr_write_b32 a225, 0\n"
"v_accvgpr_write_b32 a226, 0\n"
"v_accvgpr_write_b32 a227, 0\n"
"v_accvgpr_write_b32 a228, 0\n"
"v_accvgpr_write_b32 a229, 0\n"
"v_accvgpr_write_b32 a230, 0\n"
"v_accvgpr_write_b32 a231, 0\n"
"v_accvgpr_write_b32 a232, 0\n"
"v_accvgpr_write_b32 a233, 0\n"
"v_accvgpr_write_b32 a234, 0\n"
"v_accvgpr_write_b32 a235, 0\n"
"v_accvgpr_write_b32 a236, 0\n"
"v_accvgpr_write_b32 a237, 0\n"
"v_accvgpr_write_b32 a238, 0\n"
"v_accvgpr_write_b32 a239, 0\n"
"v_accvgpr_write_b32 a240, 0\n"
"v_accvgpr_write_b32 a241, 0\n"
"v_accvgpr_write_b32 a242, 0\n"
"v_accvgpr_write_b32 a243, 0\n"
"v_accvgpr_write_b32 a244, 0\n"
"v_accvgpr_write_b32 a245, 0\n"
"v_accvgpr_write_b32 a246, 0\n"
"v_accvgpr_write_b32 a247, 0\n"
"v_accvgpr_write_b32 a248, 0\n"
"v_accvgpr_write_b32 a249, 0\n"
"v_accvgpr_write_b32 a250, 0\n"
"v_accvgpr_write_b32 a251, 0\n"
"v_accvgpr_write_b32 a252, 0\n"
"v_accvgpr_write_b32 a253, 0\n"
"v_accvgpr_write_b32 a254, 0\n"
"v_accvgpr_write_b32 a255, 0\n"
"s_waitcnt vmcnt(35)\n"
"ds_read_b128 v[0:3], v240 offset:0\n"
"ds_read_b128 v[4:7], v241 offset:0\n"
"ds_read_b128 v[8:11], v242 offset:0\n"
"ds_read_b128 v[12:15], v243 offset:0\n"
"s_waitcnt vmcnt(31)\n"
"ds_read_b128 v[16:19], v240 offset:1024\n"
"ds_read_b128 v[20:23], v241 offset:1024\n"
"ds_read_b128 v[24:27], v242 offset:1024\n"
"ds_read_b128 v[28:31], v243 offset:1024\n"
"s_waitcnt lgkmcnt(4)\n"
"v_mfma_f32_32x32x16_bf16 v[144:159], v[224:227], v[0:3], 0\n"
"v_mfma_f32_32x32x16_bf16 v[160:175], v[0:3], v[0:3], 0\n"
"v_mfma_f32_32x32x16_bf16 v[144:159], v[224:227], v[4:7], v[144:159]\n"
"v_mfma_f32_32x32x16_bf16 v[160:175], v[4:7], v[4:7], v[160:175]\n"
"v_mfma_f32_32x32x16_bf16 v[144:159], v[224:227], v[8:11], v[144:159]\n"
"v_mfma_f32_32x32x16_bf16 v[160:175], v[8:11], v[8:11], v[160:175]\n"
"v_mfma_f32_32x32x16_bf16 v[144:159], v[224:227], v[12:15], v[144:159]\n"
"v_mfma_f32_32x32x16_bf16 v[160:175], v[12:15], v[12:15], v[160:175]\n"
"s_waitcnt vmcnt(27)\n"
"ds_read_b128 v[32:35], v240 offset:2048\n"
"ds_read_b128 v[36:39], v241 offset:2048\n"
"ds_read_b128 v[40:43], v242 offset:2048\n"
"ds_read_b128 v[44:47], v243 offset:2048\n"
"s_waitcnt lgkmcnt(4)\n"
"v_mfma_f32_32x32x16_bf16 v[144:159], v[224:227], v[16:19], v[144:159]\n"
"v_mfma_f32_32x32x16_bf16 v[160:175], v[16:19], v[16:19], v[160:175]\n"
"v_mfma_f32_32x32x16_bf16 v[144:159], v[224:227], v[20:23], v[144:159]\n"
"v_mfma_f32_32x32x16_bf16 v[160:175], v[20:23], v[20:23], v[160:175]\n"
"v_mfma_f32_32x32x16_bf16 v[144:159], v[224:227], v[24:27], v[144:159]\n"
"v_mfma_f32_32x32x16_bf16 v[160:175], v[24:27], v[24:27], v[160:175]\n"
"v_mfma_f32_32x32x16_bf16 v[144:159], v[224:227], v[28:31], v[144:159]\n"
"v_mfma_f32_32x32x16_bf16 v[160:175], v[28:31], v[28:31], v[160:175]\n"
"s_waitcnt vmcnt(23)\n"
"ds_read_b128 v[48:51], v240 offset:3072\n"
"ds_read_b128 v[52:55], v241 offset:3072\n"
"ds_read_b128 v[56:59], v242 offset:3072\n"
"ds_read_b128 v[60:63], v243 offset:3072\n"
"s_waitcnt lgkmcnt(4)\n"
"v_mfma_f32_32x32x16_bf16 v[144:159], v[224:227], v[32:35], v[144:159]\n"
"v_mfma_f32_32x32x16_bf16 v[160:175], v[32:35], v[32:35], v[160:175]\n"
"v_mfma_f32_32x32x16_bf16 v[144:159], v[224:227], v[36:39], v[144:159]\n"
"v_mfma_f32_32x32x16_bf16 v[160:175], v[36:39], v[36:39], v[160:175]\n"
"v_mfma_f32_32x32x16_bf16 v[144:159], v[224:227], v[40:43], v[144:159]\n"
"v_mfma_f32_32x32x16_bf16 v[160:175], v[40:43], v[40:43], v[160:175]\n"
"v_mfma_f32_32x32x16_bf16 v[144:159], v[224:227], v[44:47], v[144:159]\n"
"v_mfma_f32_32x32x16_bf16 v[160:175], v[44:47], v[44:47], v[160:175]\n"
"s_waitcnt vmcnt(19)\n"
"ds_read_b128 v[64:67], v240 offset:4096\n"
"ds_read_b128 v[68:71], v241 offset:4096\n"
"ds_read_b128 v[72:75], v242 offset:4096\n"
"ds_read_b128 v[76:79], v243 offset:4096\n"
"s_waitcnt lgkmcnt(4)\n"
"v_mfma_f32_32x32x16_bf16 v[144:159], v[224:227], v[48:51], v[144:159]\n"
"v_mfma_f32_32x32x16_bf16 v[160:175], v[48:51], v[48:51], v[160:175]\n"
"v_mfma_f32_32x32x16_bf16 v[144:159], v[224:227], v[52:55], v[144:159]\n"
"v_mfma_f32_32x32x16_bf16 v[160:175], v[52:55], v[52:55], v[160:175]\n"
"v_mfma_f32_32x32x16_bf16 v[144:159], v[224:227], v[56:59], v[144:159]\n"
"v_mfma_f32_32x32x16_bf16 v[160:175], v[56:59], v[56:59], v[160:175]\n"
"v_mfma_f32_32x32x16_bf16 v[144:159], v[224:227], v[60:63], v[144:159]\n"
"v_mfma_f32_32x32x16_bf16 v[160:175], v[60:63], v[60:63], v[160:175]\n"
"s_waitcnt vmcnt(15)\n"
"ds_read_b128 v[80:83], v240 offset:5120\n"
"ds_read_b128 v[84:87], v241 offset:5120\n"
"ds_read_b128 v[88:91], v242 offset:5120\n"
"ds_read_b128 v[92:95], v243 offset:5120\n"
"s_waitcnt lgkmcnt(4)\n"
"v_mfma_f32_32x32x16_bf16 v[144:159], v[224:227], v[64:67], v[144:159]\n"
"v_mfma_f32_32x32x16_bf16 v[160:175], v[64:67], v[64:67], v[160:175]\n"
"v_mfma_f32_32x32x16_bf16 v[144:159], v[224:227], v[68:71], v[144:159]\n"
"v_mfma_f32_32x32x16_bf16 v[160:175], v[68:71], v[68:71], v[160:175]\n"
"v_mfma_f32_32x32x16_bf16 v[144:159], v[224:227], v[72:75], v[144:159]\n"
"v_mfma_f32_32x32x16_bf16 v[160:175], v[72:75], v[72:75], v[160:175]\n"
"v_mfma_f32_32x32x16_bf16 v[144:159], v[224:227], v[76:79], v[144:159]\n"
"v_mfma_f32_32x32x16_bf16 v[160:175], v[76:79], v[76:79], v[160:175]\n"
"s_waitcnt vmcnt(11)\n"
"ds_read_b128 v[96:99], v240 offset:6144\n"
"ds_read_b128 v[100:103], v241 offset:6144\n"
"ds_read_b128 v[104:107], v242 offset:6144\n"
"ds_read_b128 v[108:111], v243 offset:6144\n"
"s_waitcnt lgkmcnt(4)\n"
"v_mfma_f32_32x32x16_bf16 v[144:159], v[224:227], v[80:83], v[144:159]\n"
"v_mfma_f32_32x32x16_bf16 v[160:175], v[80:83], v[80:83], v[160:175]\n"
"v_mfma_f32_32x32x16_bf16 v[144:159], v[224:227], v[84:87], v[144:159]\n"
"v_mfma_f32_32x32x16_bf16 v[160:175], v[84:87], v[84:87], v[160:175]\n"
"v_mfma_f32_32x32x16_bf16 v[144:159], v[224:227], v[88:91], v[144:159]\n"
"v_mfma_f32_32x32x16_bf16 v[160:175], v[88:91], v[88:91], v[160:175]\n"
"v_mfma_f32_32x32x16_bf16 v[144:159], v[224:227], v[92:95], v[144:159]\n"
"v_mfma_f32_32x32x16_bf16 v[160:175], v[92:95], v[92:95], v[160:175]\n"
"s_waitcnt vmcnt(7)\n"
"ds_read_b128 v[112:115], v240 offset:7168\n"
"ds_read_b128 v[116:119], v241 offset:7168\n"
"ds_read_b128 v[120:123], v242 offset:7168\n"
"ds_read_b128 v[124:127], v243 offset:7168\n"
"s_waitcnt lgkmcnt(4)\n"
"v_mfma_f32_32x32x16_bf16 v[144:159], v[224:227], v[96:99], v[144:159]\n"
"v_mfma_f32_32x32x16_bf16 v[160:175], v[96:99], v[96:99], v[160:175]\n"
"v_mfma_f32_32x32x16_bf16 v[144:159], v[224:227], v[100:103], v[144:159]\n"
"v_mfma_f32_32x32x16_bf16 v[160:175], v[100:103], v[100:103], v[160:175]\n"
"v_mfma_f32_32x32x16_bf16 v[144:159], v[224:227], v[104:107], v[144:159]\n"
"v_mfma_f32_32x32x16_bf16 v[160:175], v[104:107], v[104:107], v[160:175]\n"
"v_mfma_f32_32x32x16_bf16 v[144:159], v[224:227], v[108:111], v[144:159]\n"
"v_mfma_f32_32x32x16_bf16 v[160:175], v[108:111], v[108:111], v[160:175]\n"
"s_waitcnt vmcnt(3)\n"
"ds_read_b128 v[128:131], v240 offset:8192\n"
"ds_read_b128 v[132:135], v241 offset:8192\n"
"ds_read_b128 v[136:139], v242 offset:8192\n"
"ds_read_b128 v[140:143], v243 offset:8192\n"
"s_waitcnt lgkmcnt(4)\n"
"v_mfma_f32_32x32x16_bf16 v[144:159], v[224:227], v[112:115], v[144:159]\n"
"v_mfma_f32_32x32x16_bf16 v[160:175], v[112:115], v[112:115], v[160:175]\n"
"v_mfma_f32_32x32x16_bf16 v[144:159], v[224:227], v[116:119], v[144:159]\n"
"v_mfma_f32_32x32x16_bf16 v[160:175], v[116:119], v[116:119], v[160:175]\n"
"v_mfma_f32_32x32x16_bf16 v[144:159], v[224:227], v[120:123], v[144:159]\n"
"v_mfma_f32_32x32x16_bf16 v[160:175], v[120:123], v[120:123], v[160:175]\n"
"v_mfma_f32_32x32x16_bf16 v[144:159], v[224:227], v[124:127], v[144:159]\n"
"v_mfma_f32_32x32x16_bf16 v[160:175], v[124:127], v[124:127], v[160:175]\n"
"s_waitcnt lgkmcnt(0)\n"
"s_barrier\n"
"v_mfma_f32_32x32x16_bf16 v[144:159], v[224:227], v[128:131], v[144:159]\n"
"v_mfma_f32_32x32x16_bf16 v[160:175], v[128:131], v[128:131], v[160:175]\n"
"v_mfma_f32_32x32x16_bf16 v[144:159], v[224:227], v[132:135], v[144:159]\n"
"v_mfma_f32_32x32x16_bf16 v[160:175], v[132:135], v[132:135], v[160:175]\n"
"v_mfma_f32_32x32x16_bf16 v[144:159], v[224:227], v[136:139], v[144:159]\n"
"v_mfma_f32_32x32x16_bf16 v[160:175], v[136:139], v[136:139], v[160:175]\n"
"v_mfma_f32_32x32x16_bf16 v[144:159], v[224:227], v[140:143], v[144:159]\n"
"v_mfma_f32_32x32x16_bf16 v[160:175], v[140:143], v[140:143], v[160:175]\n"
"s_add_u32 m0, s52, 0\n"
"s_nop 0\n"
"buffer_load_dwordx4 v222, s[40:43], 0 offen lds\n"
"s_add_u32 m0, s52, 4352\n"
"s_nop 0\n"
"buffer_load_dwordx4 v222, s[40:43], s56 offen lds\n"
"s_add_u32 m0, s52, 8704\n"
"s_nop 0\n"
"buffer_load_dwordx4 v222, s[40:43], s57 offen lds\n"
"s_add_u32 m0, s52, 13056\n"
"s_nop 0\n"
"buffer_load_dwordx4 v222, s[40:43], s58 offen lds\n"
"s_add_u32 m0, s52, 17408\n"
"s_nop 0\n"
"buffer_load_dwordx4 v222, s[40:43], s59 offen lds\n"
"s_add_u32 m0, s52, 21760\n"
"s_nop 0\n"
"buffer_load_dwordx4 v222, s[40:43], s60 offen lds\n"
"s_add_u32 m0, s52, 26112\n"
"s_nop 0\n"
"buffer_load_dwordx4 v222, s[40:43], s61 offen lds\n"
"s_add_u32 m0, s52, 30464\n"
"s_nop 0\n"
"buffer_load_dwordx4 v222, s[40:43], s62 offen lds\n"
"s_add_u32 m0, s52, 34816\n"
"s_nop 0\n"
"buffer_load_dwordx4 v222, s[40:43], s63 offen lds\n"
"s_add_u32 m0, s53, 0\n"
"s_nop 0\n"
"buffer_load_dwordx4 v222, s[40:43], s64 offen lds\n"
"s_add_u32 m0, s53, 4352\n"
"s_nop 0\n"
"buffer_load_dwordx4 v222, s[40:43], s65 offen lds\n"
"s_add_u32 m0, s53, 8704\n"
"s_nop 0\n"
"buffer_load_dwordx4 v222, s[40:43], s66 offen lds\n"
"s_add_u32 m0, s53, 13056\n"
"s_nop 0\n"
"buffer_load_dwordx4 v222, s[40:43], s67 offen lds\n"
"s_add_u32 m0, s53, 17408\n"
"s_nop 0\n"
"buffer_load_dwordx4 v222, s[40:43], s68 offen lds\n"
"s_add_u32 m0, s53, 21760\n"
"s_nop 0\n"
"buffer_load_dwordx4 v222, s[40:43], s69 offen lds\n"
"s_add_u32 m0, s53, 26112\n"
"s_nop 0\n"
"buffer_load_dwordx4 v222, s[40:43], s70 offen lds\n"
"s_add_u32 m0, s53, 30464\n"
"s_nop 0\n"
"buffer_load_dwordx4 v222, s[40:43], s71 offen lds\n"
"s_add_u32 m0, s53, 34816\n"
"s_nop 0\n"
"buffer_load_dwordx4 v222, s[40:43], s72 offen lds\n"
"s_add_u32 m0, s54, 0\n"
"s_nop 0\n"
"buffer_load_dwordx4 v223, s[48:51], 0 offen lds\n"
"s_add_u32 m0, s54, 4224\n"
"s_nop 0\n"
"buffer_load_dwordx4 v223, s[48:51], 0 offen offset:128 lds\n"
"s_add_u32 m0, s54, 8448\n"
"s_nop 0\n"
"buffer_load_dwordx4 v223, s[48:51], 0 offen offset:256 lds\n"
"s_add_u32 m0, s54, 12672\n"
"s_nop 0\n"
"buffer_load_dwordx4 v223, s[48:51], 0 offen offset:384 lds\n"
"s_add_u32 m0, s54, 16896\n"
"s_nop 0\n"
"buffer_load_dwordx4 v223, s[48:51], 0 offen offset:512 lds\n"
"s_add_u32 m0, s54, 21120\n"
"s_nop 0\n"
"buffer_load_dwordx4 v223, s[48:51], 0 offen offset:640 lds\n"
"s_add_u32 m0, s54, 25344\n"
"s_nop 0\n"
"buffer_load_dwordx4 v223, s[48:51], 0 offen offset:768 lds\n"
"s_add_u32 m0, s54, 29568\n"
"s_nop 0\n"
"buffer_load_dwordx4 v223, s[48:51], 0 offen offset:896 lds\n"
"s_add_u32 m0, s54, 33792\n"
"s_nop 0\n"
"buffer_load_dwordx4 v223, s[48:51], 0 offen offset:1024 lds\n"
"v_and_b32 v248, 31, v192\n"
"v_and_b32 v249, 1, v248\n"
"v_cmp_ne_u32 s[84:85], 0, v249\n"
"v_and_b32 v249, 2, v248\n"
"v_cmp_ne_u32 s[86:87], 0, v249\n"
"v_and_b32 v249, 8, v248\n"
"v_cmp_ne_u32 s[80:81], 0, v249\n"
"s_nop 7\n"
"s_nop 7\n"
"v_cndmask_b32 v176, v160, v161, s[84:85]\n"
"v_cndmask_b32 v177, v162, v163, s[84:85]\n"
"v_cndmask_b32 v178, v164, v165, s[84:85]\n"
"v_cndmask_b32 v179, v166, v167, s[84:85]\n"
"v_cndmask_b32 v180, v168, v169, s[84:85]\n"
"v_cndmask_b32 v181, v170, v171, s[84:85]\n"
"v_cndmask_b32 v182, v172, v173, s[84:85]\n"
"v_cndmask_b32 v183, v174, v175, s[84:85]\n"
"v_cndmask_b32 v184, v176, v177, s[86:87]\n"
"v_cndmask_b32 v185, v178, v179, s[86:87]\n"
"v_cndmask_b32 v186, v180, v181, s[86:87]\n"
"v_cndmask_b32 v187, v182, v183, s[86:87]\n"
"v_cndmask_b32 v188, v184, v185, s[80:81]\n"
"v_cndmask_b32 v189, v186, v187, s[80:81]\n"
"v_and_b32 v249, 16, v248\n"
"v_cmp_ne_u32 vcc, 0, v249\n"
"s_nop 1\n"
"v_cndmask_b32 v190, v188, v189, vcc\n"
"v_mov_b32 v191, v190\n"
"v_and_b32 v249, 4, v248\n"
"v_cmp_ne_u32 vcc, 0, v249\n"
"s_nop 1\n"
"v_permlane32_swap_b32 v190, v191\n"
"s_nop 1\n"
"v_cndmask_b32 v250, v190, v191, vcc\n"
"v_mul_f32 v251, 0x3ae38e39, v144\n"
"v_mul_f32 v252, v251, v251\n"
"v_mov_b32 v253, 0x3ae38e39\n"
"v_fma_f32 v250, v250, v253, -v252\n"
"v_max_f32 v250, 0, v250\n"
"v_add_f32 v250, 0x3727c5ac, v250\n"
"v_rsq_f32 v250, v250\n"
"s_nop 0\n"
"v_mul_f32 v250, 0x3fb8aa3b, v250\n"
"v_sub_f32 v196, 0, v251\n"
"v_sub_f32 v197, 0, v251\n"
"v_sub_f32 v198, 0, v251\n"
"v_sub_f32 v199, 0, v251\n"
"v_sub_f32 v200, 0, v251\n"
"v_sub_f32 v201, 0, v251\n"
"v_sub_f32 v202, 0, v251\n"
"v_sub_f32 v203, 0, v251\n"
"v_sub_f32 v204, 0, v251\n"
"v_sub_f32 v205, 0, v251\n"
"v_sub_f32 v206, 0, v251\n"
"v_sub_f32 v207, 0, v251\n"
"v_sub_f32 v208, 0, v251\n"
"v_sub_f32 v209, 0, v251\n"
"v_sub_f32 v210, 0, v251\n"
"v_sub_f32 v211, 0, v251\n"
"s_nop 1\n"
"v_mfma_f32_32x32x16_bf16 v[144:159], v[228:231], v[0:3], v[196:211]\n"
"v_mfma_f32_32x32x16_bf16 v[144:159], v[232:235], v[4:7], v[144:159]\n"
"v_mfma_f32_32x32x16_bf16 v[160:175], v[228:231], v[8:11], v[196:211]\n"
"v_mfma_f32_32x32x16_bf16 v[160:175], v[232:235], v[12:15], v[160:175]\n"
"s_nop 7\n"
"v_mfma_f32_32x32x16_bf16 v[176:191], v[228:231], v[16:19], v[196:211]\n"
"v_mfma_f32_32x32x16_bf16 v[176:191], v[232:235], v[20:23], v[176:191]\n"
"v_mul_f32 v144, v144, v250\n"
"v_mul_f32 v145, v145, v250\n"
"v_mul_f32 v146, v146, v250\n"
"v_mul_f32 v147, v147, v250\n"
"v_mul_f32 v148, v148, v250\n"
"v_mul_f32 v149, v149, v250\n"
"v_mul_f32 v150, v150, v250\n"
"v_mul_f32 v151, v151, v250\n"
"v_mul_f32 v152, v152, v250\n"
"v_mul_f32 v153, v153, v250\n"
"v_mul_f32 v154, v154, v250\n"
"v_mul_f32 v155, v155, v250\n"
"v_mul_f32 v156, v156, v250\n"
"v_mul_f32 v157, v157, v250\n"
"v_mul_f32 v158, v158, v250\n"
"v_mul_f32 v159, v159, v250\n"
"v_cvt_pk_bf16_f32 v0, v144, v145\n"
"v_cvt_pk_bf16_f32 v1, v146, v147\n"
"v_cvt_pk_bf16_f32 v2, v148, v149\n"
"v_cvt_pk_bf16_f32 v3, v150, v151\n"
"v_cvt_pk_bf16_f32 v4, v152, v153\n"
"v_cvt_pk_bf16_f32 v5, v154, v155\n"
"v_cvt_pk_bf16_f32 v6, v156, v157\n"
"v_cvt_pk_bf16_f32 v7, v158, v159\n"
"v_mfma_f32_32x32x16_bf16 v[144:159], v[228:231], v[24:27], v[196:211]\n"
"v_mfma_f32_32x32x16_bf16 v[144:159], v[232:235], v[28:31], v[144:159]\n"
"v_mul_f32 v160, v160, v250\n"
"v_mul_f32 v161, v161, v250\n"
"v_mul_f32 v162, v162, v250\n"
"v_mul_f32 v163, v163, v250\n"
"v_mul_f32 v164, v164, v250\n"
"v_mul_f32 v165, v165, v250\n"
"v_mul_f32 v166, v166, v250\n"
"v_mul_f32 v167, v167, v250\n"
"v_mul_f32 v168, v168, v250\n"
"v_mul_f32 v169, v169, v250\n"
"v_mul_f32 v170, v170, v250\n"
"v_mul_f32 v171, v171, v250\n"
"v_mul_f32 v172, v172, v250\n"
"v_mul_f32 v173, v173, v250\n"
"v_mul_f32 v174, v174, v250\n"
"v_mul_f32 v175, v175, v250\n"
"v_cvt_pk_bf16_f32 v8, v160, v161\n"
"v_cvt_pk_bf16_f32 v9, v162, v163\n"
"v_cvt_pk_bf16_f32 v10, v164, v165\n"
"v_cvt_pk_bf16_f32 v11, v166, v167\n"
"v_cvt_pk_bf16_f32 v12, v168, v169\n"
"v_cvt_pk_bf16_f32 v13, v170, v171\n"
"v_cvt_pk_bf16_f32 v14, v172, v173\n"
"v_cvt_pk_bf16_f32 v15, v174, v175\n"
"v_mfma_f32_32x32x16_bf16 v[160:175], v[228:231], v[32:35], v[196:211]\n"
"v_mfma_f32_32x32x16_bf16 v[160:175], v[232:235], v[36:39], v[160:175]\n"
"v_mul_f32 v176, v176, v250\n"
"v_mul_f32 v177, v177, v250\n"
"v_mul_f32 v178, v178, v250\n"
"v_mul_f32 v179, v179, v250\n"
"v_mul_f32 v180, v180, v250\n"
"v_mul_f32 v181, v181, v250\n"
"v_mul_f32 v182, v182, v250\n"
"v_mul_f32 v183, v183, v250\n"
"v_mul_f32 v184, v184, v250\n"
"v_mul_f32 v185, v185, v250\n"
"v_mul_f32 v186, v186, v250\n"
"v_mul_f32 v187, v187, v250\n"
"v_mul_f32 v188, v188, v250\n"
"v_mul_f32 v189, v189, v250\n"
"v_mul_f32 v190, v190, v250\n"
"v_mul_f32 v191, v191, v250\n"
"v_cvt_pk_bf16_f32 v16, v176, v177\n"
"v_cvt_pk_bf16_f32 v17, v178, v179\n"
"v_cvt_pk_bf16_f32 v18, v180, v181\n"
"v_cvt_pk_bf16_f32 v19, v182, v183\n"
"v_cvt_pk_bf16_f32 v20, v184, v185\n"
"v_cvt_pk_bf16_f32 v21, v186, v187\n"
"v_cvt_pk_bf16_f32 v22, v188, v189\n"
"v_cvt_pk_bf16_f32 v23, v190, v191\n"
"v_mfma_f32_32x32x16_bf16 v[176:191], v[228:231], v[40:43], v[196:211]\n"
"v_mfma_f32_32x32x16_bf16 v[176:191], v[232:235], v[44:47], v[176:191]\n"
"v_mul_f32 v144, v144, v250\n"
"v_mul_f32 v145, v145, v250\n"
"v_mul_f32 v146, v146, v250\n"
"v_mul_f32 v147, v147, v250\n"
"v_mul_f32 v148, v148, v250\n"
"v_mul_f32 v149, v149, v250\n"
"v_mul_f32 v150, v150, v250\n"
"v_mul_f32 v151, v151, v250\n"
"v_mul_f32 v152, v152, v250\n"
"v_mul_f32 v153, v153, v250\n"
"v_mul_f32 v154, v154, v250\n"
"v_mul_f32 v155, v155, v250\n"
"v_mul_f32 v156, v156, v250\n"
"v_mul_f32 v157, v157, v250\n"
"v_mul_f32 v158, v158, v250\n"
"v_mul_f32 v159, v159, v250\n"
"v_cvt_pk_bf16_f32 v24, v144, v145\n"
"v_cvt_pk_bf16_f32 v25, v146, v147\n"
"v_cvt_pk_bf16_f32 v26, v148, v149\n"
"v_cvt_pk_bf16_f32 v27, v150, v151\n"
"v_cvt_pk_bf16_f32 v28, v152, v153\n"
"v_cvt_pk_bf16_f32 v29, v154, v155\n"
"v_cvt_pk_bf16_f32 v30, v156, v157\n"
"v_cvt_pk_bf16_f32 v31, v158, v159\n"
"v_mfma_f32_32x32x16_bf16 v[144:159], v[228:231], v[48:51], v[196:211]\n"
"v_mfma_f32_32x32x16_bf16 v[144:159], v[232:235], v[52:55], v[144:159]\n"
"v_mul_f32 v160, v160, v250\n"
"v_mul_f32 v161, v161, v250\n"
"v_mul_f32 v162, v162, v250\n"
"v_mul_f32 v163, v163, v250\n"
"v_mul_f32 v164, v164, v250\n"
"v_mul_f32 v165, v165, v250\n"
"v_mul_f32 v166, v166, v250\n"
"v_mul_f32 v167, v167, v250\n"
"v_mul_f32 v168, v168, v250\n"
"v_mul_f32 v169, v169, v250\n"
"v_mul_f32 v170, v170, v250\n"
"v_mul_f32 v171, v171, v250\n"
"v_mul_f32 v172, v172, v250\n"
"v_mul_f32 v173, v173, v250\n"
"v_mul_f32 v174, v174, v250\n"
"v_mul_f32 v175, v175, v250\n"
"v_cvt_pk_bf16_f32 v32, v160, v161\n"
"v_cvt_pk_bf16_f32 v33, v162, v163\n"
"v_cvt_pk_bf16_f32 v34, v164, v165\n"
"v_cvt_pk_bf16_f32 v35, v166, v167\n"
"v_cvt_pk_bf16_f32 v36, v168, v169\n"
"v_cvt_pk_bf16_f32 v37, v170, v171\n"
"v_cvt_pk_bf16_f32 v38, v172, v173\n"
"v_cvt_pk_bf16_f32 v39, v174, v175\n"
"v_mfma_f32_32x32x16_bf16 v[160:175], v[228:231], v[56:59], v[196:211]\n"
"v_mfma_f32_32x32x16_bf16 v[160:175], v[232:235], v[60:63], v[160:175]\n"
"v_mul_f32 v176, v176, v250\n"
"v_mul_f32 v177, v177, v250\n"
"v_mul_f32 v178, v178, v250\n"
"v_mul_f32 v179, v179, v250\n"
"v_mul_f32 v180, v180, v250\n"
"v_mul_f32 v181, v181, v250\n"
"v_mul_f32 v182, v182, v250\n"
"v_mul_f32 v183, v183, v250\n"
"v_mul_f32 v184, v184, v250\n"
"v_mul_f32 v185, v185, v250\n"
"v_mul_f32 v186, v186, v250\n"
"v_mul_f32 v187, v187, v250\n"
"v_mul_f32 v188, v188, v250\n"
"v_mul_f32 v189, v189, v250\n"
"v_mul_f32 v190, v190, v250\n"
"v_mul_f32 v191, v191, v250\n"
"v_cvt_pk_bf16_f32 v40, v176, v177\n"
"v_cvt_pk_bf16_f32 v41, v178, v179\n"
"v_cvt_pk_bf16_f32 v42, v180, v181\n"
"v_cvt_pk_bf16_f32 v43, v182, v183\n"
"v_cvt_pk_bf16_f32 v44, v184, v185\n"
"v_cvt_pk_bf16_f32 v45, v186, v187\n"
"v_cvt_pk_bf16_f32 v46, v188, v189\n"
"v_cvt_pk_bf16_f32 v47, v190, v191\n"
"v_mfma_f32_32x32x16_bf16 v[176:191], v[228:231], v[64:67], v[196:211]\n"
"v_mfma_f32_32x32x16_bf16 v[176:191], v[232:235], v[68:71], v[176:191]\n"
"v_mul_f32 v144, v144, v250\n"
"v_mul_f32 v145, v145, v250\n"
"v_mul_f32 v146, v146, v250\n"
"v_mul_f32 v147, v147, v250\n"
"v_mul_f32 v148, v148, v250\n"
"v_mul_f32 v149, v149, v250\n"
"v_mul_f32 v150, v150, v250\n"
"v_mul_f32 v151, v151, v250\n"
"v_mul_f32 v152, v152, v250\n"
"v_mul_f32 v153, v153, v250\n"
"v_mul_f32 v154, v154, v250\n"
"v_mul_f32 v155, v155, v250\n"
"v_mul_f32 v156, v156, v250\n"
"v_mul_f32 v157, v157, v250\n"
"v_mul_f32 v158, v158, v250\n"
"v_mul_f32 v159, v159, v250\n"
"v_cvt_pk_bf16_f32 v48, v144, v145\n"
"v_cvt_pk_bf16_f32 v49, v146, v147\n"
"v_cvt_pk_bf16_f32 v50, v148, v149\n"
"v_cvt_pk_bf16_f32 v51, v150, v151\n"
"v_cvt_pk_bf16_f32 v52, v152, v153\n"
"v_cvt_pk_bf16_f32 v53, v154, v155\n"
"v_cvt_pk_bf16_f32 v54, v156, v157\n"
"v_cvt_pk_bf16_f32 v55, v158, v159\n"
"v_mfma_f32_32x32x16_bf16 v[144:159], v[228:231], v[72:75], v[196:211]\n"
"v_mfma_f32_32x32x16_bf16 v[144:159], v[232:235], v[76:79], v[144:159]\n"
"v_mul_f32 v160, v160, v250\n"
"v_mul_f32 v161, v161, v250\n"
"v_mul_f32 v162, v162, v250\n"
"v_mul_f32 v163, v163, v250\n"
"v_mul_f32 v164, v164, v250\n"
"v_mul_f32 v165, v165, v250\n"
"v_mul_f32 v166, v166, v250\n"
"v_mul_f32 v167, v167, v250\n"
"v_mul_f32 v168, v168, v250\n"
"v_mul_f32 v169, v169, v250\n"
"v_mul_f32 v170, v170, v250\n"
"v_mul_f32 v171, v171, v250\n"
"v_mul_f32 v172, v172, v250\n"
"v_mul_f32 v173, v173, v250\n"
"v_mul_f32 v174, v174, v250\n"
"v_mul_f32 v175, v175, v250\n"
"v_cvt_pk_bf16_f32 v56, v160, v161\n"
"v_cvt_pk_bf16_f32 v57, v162, v163\n"
"v_cvt_pk_bf16_f32 v58, v164, v165\n"
"v_cvt_pk_bf16_f32 v59, v166, v167\n"
"v_cvt_pk_bf16_f32 v60, v168, v169\n"
"v_cvt_pk_bf16_f32 v61, v170, v171\n"
"v_cvt_pk_bf16_f32 v62, v172, v173\n"
"v_cvt_pk_bf16_f32 v63, v174, v175\n"
"v_mfma_f32_32x32x16_bf16 v[160:175], v[228:231], v[80:83], v[196:211]\n"
"v_mfma_f32_32x32x16_bf16 v[160:175], v[232:235], v[84:87], v[160:175]\n"
"v_mul_f32 v176, v176, v250\n"
"v_mul_f32 v177, v177, v250\n"
"v_mul_f32 v178, v178, v250\n"
"v_mul_f32 v179, v179, v250\n"
"v_mul_f32 v180, v180, v250\n"
"v_mul_f32 v181, v181, v250\n"
"v_mul_f32 v182, v182, v250\n"
"v_mul_f32 v183, v183, v250\n"
"v_mul_f32 v184, v184, v250\n"
"v_mul_f32 v185, v185, v250\n"
"v_mul_f32 v186, v186, v250\n"
"v_mul_f32 v187, v187, v250\n"
"v_mul_f32 v188, v188, v250\n"
"v_mul_f32 v189, v189, v250\n"
"v_mul_f32 v190, v190, v250\n"
"v_mul_f32 v191, v191, v250\n"
"v_cvt_pk_bf16_f32 v64, v176, v177\n"
"v_cvt_pk_bf16_f32 v65, v178, v179\n"
"v_cvt_pk_bf16_f32 v66, v180, v181\n"
"v_cvt_pk_bf16_f32 v67, v182, v183\n"
"v_cvt_pk_bf16_f32 v68, v184, v185\n"
"v_cvt_pk_bf16_f32 v69, v186, v187\n"
"v_cvt_pk_bf16_f32 v70, v188, v189\n"
"v_cvt_pk_bf16_f32 v71, v190, v191\n"
"v_mfma_f32_32x32x16_bf16 v[176:191], v[228:231], v[88:91], v[196:211]\n"
"v_mfma_f32_32x32x16_bf16 v[176:191], v[232:235], v[92:95], v[176:191]\n"
"v_mul_f32 v144, v144, v250\n"
"v_mul_f32 v145, v145, v250\n"
"v_mul_f32 v146, v146, v250\n"
"v_mul_f32 v147, v147, v250\n"
"v_mul_f32 v148, v148, v250\n"
"v_mul_f32 v149, v149, v250\n"
"v_mul_f32 v150, v150, v250\n"
"v_mul_f32 v151, v151, v250\n"
"v_mul_f32 v152, v152, v250\n"
"v_mul_f32 v153, v153, v250\n"
"v_mul_f32 v154, v154, v250\n"
"v_mul_f32 v155, v155, v250\n"
"v_mul_f32 v156, v156, v250\n"
"v_mul_f32 v157, v157, v250\n"
"v_mul_f32 v158, v158, v250\n"
"v_mul_f32 v159, v159, v250\n"
"v_cvt_pk_bf16_f32 v72, v144, v145\n"
"v_cvt_pk_bf16_f32 v73, v146, v147\n"
"v_cvt_pk_bf16_f32 v74, v148, v149\n"
"v_cvt_pk_bf16_f32 v75, v150, v151\n"
"v_cvt_pk_bf16_f32 v76, v152, v153\n"
"v_cvt_pk_bf16_f32 v77, v154, v155\n"
"v_cvt_pk_bf16_f32 v78, v156, v157\n"
"v_cvt_pk_bf16_f32 v79, v158, v159\n"
"v_mfma_f32_32x32x16_bf16 v[144:159], v[228:231], v[96:99], v[196:211]\n"
"v_mfma_f32_32x32x16_bf16 v[144:159], v[232:235], v[100:103], v[144:159]\n"
"v_mul_f32 v160, v160, v250\n"
"v_mul_f32 v161, v161, v250\n"
"v_mul_f32 v162, v162, v250\n"
"v_mul_f32 v163, v163, v250\n"
"v_mul_f32 v164, v164, v250\n"
"v_mul_f32 v165, v165, v250\n"
"v_mul_f32 v166, v166, v250\n"
"v_mul_f32 v167, v167, v250\n"
"v_mul_f32 v168, v168, v250\n"
"v_mul_f32 v169, v169, v250\n"
"v_mul_f32 v170, v170, v250\n"
"v_mul_f32 v171, v171, v250\n"
"v_mul_f32 v172, v172, v250\n"
"v_mul_f32 v173, v173, v250\n"
"v_mul_f32 v174, v174, v250\n"
"v_mul_f32 v175, v175, v250\n"
"v_cvt_pk_bf16_f32 v80, v160, v161\n"
"v_cvt_pk_bf16_f32 v81, v162, v163\n"
"v_cvt_pk_bf16_f32 v82, v164, v165\n"
"v_cvt_pk_bf16_f32 v83, v166, v167\n"
"v_cvt_pk_bf16_f32 v84, v168, v169\n"
"v_cvt_pk_bf16_f32 v85, v170, v171\n"
"v_cvt_pk_bf16_f32 v86, v172, v173\n"
"v_cvt_pk_bf16_f32 v87, v174, v175\n"
"v_mfma_f32_32x32x16_bf16 v[160:175], v[228:231], v[104:107], v[196:211]\n"
"v_mfma_f32_32x32x16_bf16 v[160:175], v[232:235], v[108:111], v[160:175]\n"
"v_mul_f32 v176, v176, v250\n"
"v_mul_f32 v177, v177, v250\n"
"v_mul_f32 v178, v178, v250\n"
"v_mul_f32 v179, v179, v250\n"
"v_mul_f32 v180, v180, v250\n"
"v_mul_f32 v181, v181, v250\n"
"v_mul_f32 v182, v182, v250\n"
"v_mul_f32 v183, v183, v250\n"
"v_mul_f32 v184, v184, v250\n"
"v_mul_f32 v185, v185, v250\n"
"v_mul_f32 v186, v186, v250\n"
"v_mul_f32 v187, v187, v250\n"
"v_mul_f32 v188, v188, v250\n"
"v_mul_f32 v189, v189, v250\n"
"v_mul_f32 v190, v190, v250\n"
"v_mul_f32 v191, v191, v250\n"
"v_cvt_pk_bf16_f32 v88, v176, v177\n"
"v_cvt_pk_bf16_f32 v89, v178, v179\n"
"v_cvt_pk_bf16_f32 v90, v180, v181\n"
"v_cvt_pk_bf16_f32 v91, v182, v183\n"
"v_cvt_pk_bf16_f32 v92, v184, v185\n"
"v_cvt_pk_bf16_f32 v93, v186, v187\n"
"v_cvt_pk_bf16_f32 v94, v188, v189\n"
"v_cvt_pk_bf16_f32 v95, v190, v191\n"
"v_mfma_f32_32x32x16_bf16 v[176:191], v[228:231], v[112:115], v[196:211]\n"
"v_mfma_f32_32x32x16_bf16 v[176:191], v[232:235], v[116:119], v[176:191]\n"
"v_mul_f32 v144, v144, v250\n"
"v_mul_f32 v145, v145, v250\n"
"v_mul_f32 v146, v146, v250\n"
"v_mul_f32 v147, v147, v250\n"
"v_mul_f32 v148, v148, v250\n"
"v_mul_f32 v149, v149, v250\n"
"v_mul_f32 v150, v150, v250\n"
"v_mul_f32 v151, v151, v250\n"
"v_mul_f32 v152, v152, v250\n"
"v_mul_f32 v153, v153, v250\n"
"v_mul_f32 v154, v154, v250\n"
"v_mul_f32 v155, v155, v250\n"
"v_mul_f32 v156, v156, v250\n"
"v_mul_f32 v157, v157, v250\n"
"v_mul_f32 v158, v158, v250\n"
"v_mul_f32 v159, v159, v250\n"
"v_cvt_pk_bf16_f32 v96, v144, v145\n"
"v_cvt_pk_bf16_f32 v97, v146, v147\n"
"v_cvt_pk_bf16_f32 v98, v148, v149\n"
"v_cvt_pk_bf16_f32 v99, v150, v151\n"
"v_cvt_pk_bf16_f32 v100, v152, v153\n"
"v_cvt_pk_bf16_f32 v101, v154, v155\n"
"v_cvt_pk_bf16_f32 v102, v156, v157\n"
"v_cvt_pk_bf16_f32 v103, v158, v159\n"
"v_mfma_f32_32x32x16_bf16 v[144:159], v[228:231], v[120:123], v[196:211]\n"
"v_mfma_f32_32x32x16_bf16 v[144:159], v[232:235], v[124:127], v[144:159]\n"
"v_mul_f32 v160, v160, v250\n"
"v_mul_f32 v161, v161, v250\n"
"v_mul_f32 v162, v162, v250\n"
"v_mul_f32 v163, v163, v250\n"
"v_mul_f32 v164, v164, v250\n"
"v_mul_f32 v165, v165, v250\n"
"v_mul_f32 v166, v166, v250\n"
"v_mul_f32 v167, v167, v250\n"
"v_mul_f32 v168, v168, v250\n"
"v_mul_f32 v169, v169, v250\n"
"v_mul_f32 v170, v170, v250\n"
"v_mul_f32 v171, v171, v250\n"
"v_mul_f32 v172, v172, v250\n"
"v_mul_f32 v173, v173, v250\n"
"v_mul_f32 v174, v174, v250\n"
"v_mul_f32 v175, v175, v250\n"
"v_cvt_pk_bf16_f32 v104, v160, v161\n"
"v_cvt_pk_bf16_f32 v105, v162, v163\n"
"v_cvt_pk_bf16_f32 v106, v164, v165\n"
"v_cvt_pk_bf16_f32 v107, v166, v167\n"
"v_cvt_pk_bf16_f32 v108, v168, v169\n"
"v_cvt_pk_bf16_f32 v109, v170, v171\n"
"v_cvt_pk_bf16_f32 v110, v172, v173\n"
"v_cvt_pk_bf16_f32 v111, v174, v175\n"
"v_mfma_f32_32x32x16_bf16 v[160:175], v[228:231], v[128:131], v[196:211]\n"
"v_mfma_f32_32x32x16_bf16 v[160:175], v[232:235], v[132:135], v[160:175]\n"
"v_mul_f32 v176, v176, v250\n"
"v_mul_f32 v177, v177, v250\n"
"v_mul_f32 v178, v178, v250\n"
"v_mul_f32 v179, v179, v250\n"
"v_mul_f32 v180, v180, v250\n"
"v_mul_f32 v181, v181, v250\n"
"v_mul_f32 v182, v182, v250\n"
"v_mul_f32 v183, v183, v250\n"
"v_mul_f32 v184, v184, v250\n"
"v_mul_f32 v185, v185, v250\n"
"v_mul_f32 v186, v186, v250\n"
"v_mul_f32 v187, v187, v250\n"
"v_mul_f32 v188, v188, v250\n"
"v_mul_f32 v189, v189, v250\n"
"v_mul_f32 v190, v190, v250\n"
"v_mul_f32 v191, v191, v250\n"
"v_cvt_pk_bf16_f32 v112, v176, v177\n"
"v_cvt_pk_bf16_f32 v113, v178, v179\n"
"v_cvt_pk_bf16_f32 v114, v180, v181\n"
"v_cvt_pk_bf16_f32 v115, v182, v183\n"
"v_cvt_pk_bf16_f32 v116, v184, v185\n"
"v_cvt_pk_bf16_f32 v117, v186, v187\n"
"v_cvt_pk_bf16_f32 v118, v188, v189\n"
"v_cvt_pk_bf16_f32 v119, v190, v191\n"
"v_mfma_f32_32x32x16_bf16 v[176:191], v[228:231], v[136:139], v[196:211]\n"
"v_mfma_f32_32x32x16_bf16 v[176:191], v[232:235], v[140:143], v[176:191]\n"
"v_mul_f32 v144, v144, v250\n"
"v_mul_f32 v145, v145, v250\n"
"v_mul_f32 v146, v146, v250\n"
"v_mul_f32 v147, v147, v250\n"
"v_mul_f32 v148, v148, v250\n"
"v_mul_f32 v149, v149, v250\n"
"v_mul_f32 v150, v150, v250\n"
"v_mul_f32 v151, v151, v250\n"
"v_mul_f32 v152, v152, v250\n"
"v_mul_f32 v153, v153, v250\n"
"v_mul_f32 v154, v154, v250\n"
"v_mul_f32 v155, v155, v250\n"
"v_mul_f32 v156, v156, v250\n"
"v_mul_f32 v157, v157, v250\n"
"v_mul_f32 v158, v158, v250\n"
"v_mul_f32 v159, v159, v250\n"
"v_cvt_pk_bf16_f32 v120, v144, v145\n"
"v_cvt_pk_bf16_f32 v121, v146, v147\n"
"v_cvt_pk_bf16_f32 v122, v148, v149\n"
"v_cvt_pk_bf16_f32 v123, v150, v151\n"
"v_cvt_pk_bf16_f32 v124, v152, v153\n"
"v_cvt_pk_bf16_f32 v125, v154, v155\n"
"v_cvt_pk_bf16_f32 v126, v156, v157\n"
"v_cvt_pk_bf16_f32 v127, v158, v159\n"
"s_nop 7\n"
"v_mul_f32 v160, v160, v250\n"
"v_mul_f32 v161, v161, v250\n"
"v_mul_f32 v162, v162, v250\n"
"v_mul_f32 v163, v163, v250\n"
"v_mul_f32 v164, v164, v250\n"
"v_mul_f32 v165, v165, v250\n"
"v_mul_f32 v166, v166, v250\n"
"v_mul_f32 v167, v167, v250\n"
"v_mul_f32 v168, v168, v250\n"
"v_mul_f32 v169, v169, v250\n"
"v_mul_f32 v170, v170, v250\n"
"v_mul_f32 v171, v171, v250\n"
"v_mul_f32 v172, v172, v250\n"
"v_mul_f32 v173, v173, v250\n"
"v_mul_f32 v174, v174, v250\n"
"v_mul_f32 v175, v175, v250\n"
"v_cvt_pk_bf16_f32 v128, v160, v161\n"
"v_cvt_pk_bf16_f32 v129, v162, v163\n"
"v_cvt_pk_bf16_f32 v130, v164, v165\n"
"v_cvt_pk_bf16_f32 v131, v166, v167\n"
"v_cvt_pk_bf16_f32 v132, v168, v169\n"
"v_cvt_pk_bf16_f32 v133, v170, v171\n"
"v_cvt_pk_bf16_f32 v134, v172, v173\n"
"v_cvt_pk_bf16_f32 v135, v174, v175\n"
"s_nop 7\n"
"v_mul_f32 v176, v176, v250\n"
"v_mul_f32 v177, v177, v250\n"
"v_mul_f32 v178, v178, v250\n"
"v_mul_f32 v179, v179, v250\n"
"v_mul_f32 v180, v180, v250\n"
"v_mul_f32 v181, v181, v250\n"
"v_mul_f32 v182, v182, v250\n"
"v_mul_f32 v183, v183, v250\n"
"v_mul_f32 v184, v184, v250\n"
"v_mul_f32 v185, v185, v250\n"
"v_mul_f32 v186, v186, v250\n"
"v_mul_f32 v187, v187, v250\n"
"v_mul_f32 v188, v188, v250\n"
"v_mul_f32 v189, v189, v250\n"
"v_mul_f32 v190, v190, v250\n"
"v_mul_f32 v191, v191, v250\n"
"v_cvt_pk_bf16_f32 v136, v176, v177\n"
"v_cvt_pk_bf16_f32 v137, v178, v179\n"
"v_cvt_pk_bf16_f32 v138, v180, v181\n"
"v_cvt_pk_bf16_f32 v139, v182, v183\n"
"v_cvt_pk_bf16_f32 v140, v184, v185\n"
"v_cvt_pk_bf16_f32 v141, v186, v187\n"
"v_cvt_pk_bf16_f32 v142, v188, v189\n"
"v_cvt_pk_bf16_f32 v143, v190, v191\n"
"s_waitcnt vmcnt(18)\n"
"v_mov_b32 v224, 0\n"
"v_mov_b32 v225, 0\n"
"v_mov_b32 v226, 0\n"
"v_mov_b32 v227, 0\n"
"v_mov_b32 v228, 0\n"
"v_mov_b32 v229, 0\n"
"v_mov_b32 v230, 0\n"
"v_mov_b32 v231, 0\n"
"v_mov_b32 v232, 0\n"
"v_mov_b32 v233, 0\n"
"v_mov_b32 v234, 0\n"
"v_mov_b32 v235, 0\n"
"v_mov_b32 v236, 0\n"
"v_mov_b32 v237, 0\n"
"v_mov_b32 v238, 0\n"
"v_mov_b32 v239, 0\n"
"v_mov_b32 v240, 0\n"
"v_mov_b32 v241, 0\n"
"v_mov_b32 v242, 0\n"
"v_mov_b32 v243, 0\n"
"v_mov_b32 v244, 0\n"
"v_mov_b32 v245, 0\n"
"v_mov_b32 v246, 0\n"
"v_mov_b32 v247, 0\n"
"v_mov_b32 v248, 0\n"
"v_mov_b32 v249, 0\n"
"v_mov_b32 v250, 0\n"
"v_mov_b32 v251, 0\n"
"v_mov_b32 v252, 0\n"
"v_mov_b32 v253, 0\n"
"v_mov_b32 v254, 0\n"
"v_mov_b32 v255, 0\n"
"v_mov_b32 v160, 0\n"
"v_mov_b32 v161, 0\n"
"v_mov_b32 v162, 0\n"
"v_mov_b32 v163, 0\n"
"v_mov_b32 v164, 0\n"
"v_mov_b32 v165, 0\n"
"v_mov_b32 v166, 0\n"
"v_mov_b32 v167, 0\n"
"v_mov_b32 v168, 0\n"
"v_mov_b32 v169, 0\n"
"v_mov_b32 v170, 0\n"
"v_mov_b32 v171, 0\n"
"v_mov_b32 v172, 0\n"
"v_mov_b32 v173, 0\n"
"v_mov_b32 v174, 0\n"
"v_mov_b32 v175, 0\n"
"v_mov_b32 v184, 0\n"
"v_mov_b32 v185, 0\n"
"v_mov_b32 v186, 0\n"
"v_mov_b32 v187, 0\n"
"v_mov_b32 v188, 0\n"
"v_mov_b32 v189, 0\n"
"v_mov_b32 v190, 0\n"
"v_mov_b32 v191, 0\n"
"s_barrier\n"
"v_add_u32 v220, s52, v221\n"
"ds_read_b64_tr_b16 v[196:197], v220 offset:0\n"
"ds_read_b64_tr_b16 v[198:199], v220 offset:128\n"
"ds_read_b64_tr_b16 v[200:201], v220 offset:512\n"
"ds_read_b64_tr_b16 v[202:203], v220 offset:640\n"
"ds_read_b64_tr_b16 v[204:205], v220 offset:4352\n"
"ds_read_b64_tr_b16 v[206:207], v220 offset:4480\n"
"ds_read_b64_tr_b16 v[208:209], v220 offset:4864\n"
"ds_read_b64_tr_b16 v[210:211], v220 offset:4992\n"
"ds_read_b64_tr_b16 v[212:213], v220 offset:8704\n"
"ds_read_b64_tr_b16 v[214:215], v220 offset:8832\n"
"s_waitcnt lgkmcnt(8)\n"
"s_nop 1\n"
"1:\n"
"v_mfma_f32_32x32x16_bf16 v[144:159], v[196:199], v[0:3], 0\n"
"s_waitcnt lgkmcnt(4)\n"
"ds_read_b64_tr_b16 v[216:217], v220 offset:9216\n"
"ds_read_b64_tr_b16 v[218:219], v220 offset:9344\n"
"v_mfma_f32_32x32x16_bf16 v[144:159], v[200:203], v[4:7], v[144:159]\n"
"s_add_u32 m0, s55, 0\n"
"ds_read_b64_tr_b16 v[196:197], v220 offset:13056\n"
"ds_read_b64_tr_b16 v[198:199], v220 offset:13184\n"
"buffer_load_dwordx4 v222, s[44:47], 0 offen lds\n"
"v_mfma_f32_32x32x16_bf16 v[144:159], v[204:207], v[8:11], v[144:159]\n"
"s_waitcnt lgkmcnt(4)\n"
"ds_read_b64_tr_b16 v[200:201], v220 offset:13568\n"
"ds_read_b64_tr_b16 v[202:203], v220 offset:13696\n"
"v_mfma_f32_32x32x16_bf16 v[144:159], v[208:211], v[12:15], v[144:159]\n"
"s_add_u32 m0, s55, 4352\n"
"ds_read_b64_tr_b16 v[204:205], v220 offset:17408\n"
"ds_read_b64_tr_b16 v[206:207], v220 offset:17536\n"
"buffer_load_dwordx4 v222, s[44:47], s56 offen lds\n"
"v_mfma_f32_32x32x16_bf16 v[144:159], v[212:215], v[16:19], v[144:159]\n"
"s_waitcnt lgkmcnt(4)\n"
"ds_read_b64_tr_b16 v[208:209], v220 offset:17920\n"
"ds_read_b64_tr_b16 v[210:211], v220 offset:18048\n"
"v_mfma_f32_32x32x16_bf16 v[144:159], v[216:219], v[20:23], v[144:159]\n"
"s_add_u32 m0, s55, 8704\n"
"ds_read_b64_tr_b16 v[212:213], v220 offset:21760\n"
"ds_read_b64_tr_b16 v[214:215], v220 offset:21888\n"
"buffer_load_dwordx4 v222, s[44:47], s57 offen lds\n"
"v_mfma_f32_32x32x16_bf16 v[144:159], v[196:199], v[24:27], v[144:159]\n"
"s_waitcnt lgkmcnt(4)\n"
"ds_read_b64_tr_b16 v[216:217], v220 offset:22272\n"
"ds_read_b64_tr_b16 v[218:219], v220 offset:22400\n"
"v_mfma_f32_32x32x16_bf16 v[144:159], v[200:203], v[28:31], v[144:159]\n"
"s_add_u32 m0, s55, 13056\n"
"ds_read_b64_tr_b16 v[196:197], v220 offset:26112\n"
"ds_read_b64_tr_b16 v[198:199], v220 offset:26240\n"
"buffer_load_dwordx4 v222, s[44:47], s58 offen lds\n"
"v_mfma_f32_32x32x16_bf16 v[144:159], v[204:207], v[32:35], v[144:159]\n"
"s_waitcnt lgkmcnt(4)\n"
"ds_read_b64_tr_b16 v[200:201], v220 offset:26624\n"
"ds_read_b64_tr_b16 v[202:203], v220 offset:26752\n"
"v_mfma_f32_32x32x16_bf16 v[144:159], v[208:211], v[36:39], v[144:159]\n"
"s_add_u32 m0, s55, 17408\n"
"ds_read_b64_tr_b16 v[204:205], v220 offset:30464\n"
"ds_read_b64_tr_b16 v[206:207], v220 offset:30592\n"
"buffer_load_dwordx4 v222, s[44:47], s59 offen lds\n"
"v_mfma_f32_32x32x16_bf16 v[144:159], v[212:215], v[40:43], v[144:159]\n"
"s_waitcnt lgkmcnt(4)\n"
"ds_read_b64_tr_b16 v[208:209], v220 offset:30976\n"
"ds_read_b64_tr_b16 v[210:211], v220 offset:31104\n"
"v_mfma_f32_32x32x16_bf16 v[144:159], v[216:219], v[44:47], v[144:159]\n"
"s_add_u32 m0, s55, 21760\n"
"ds_read_b64_tr_b16 v[212:213], v220 offset:34816\n"
"ds_read_b64_tr_b16 v[214:215], v220 offset:34944\n"
"buffer_load_dwordx4 v222, s[44:47], s60 offen lds\n"
"v_mfma_f32_32x32x16_bf16 v[144:159], v[196:199], v[48:51], v[144:159]\n"
"s_waitcnt lgkmcnt(4)\n"
"ds_read_b64_tr_b16 v[216:217], v220 offset:35328\n"
"ds_read_b64_tr_b16 v[218:219], v220 offset:35456\n"
"v_mfma_f32_32x32x16_bf16 v[144:159], v[200:203], v[52:55], v[144:159]\n"
"s_add_u32 m0, s55, 26112\n"
"ds_read_b64_tr_b16 v[196:197], v220 offset:64\n"
"ds_read_b64_tr_b16 v[198:199], v220 offset:192\n"
"buffer_load_dwordx4 v222, s[44:47], s61 offen lds\n"
"v_mfma_f32_32x32x16_bf16 v[144:159], v[204:207], v[56:59], v[144:159]\n"
"s_waitcnt lgkmcnt(4)\n"
"ds_read_b64_tr_b16 v[200:201], v220 offset:576\n"
"ds_read_b64_tr_b16 v[202:203], v220 offset:704\n"
"v_mfma_f32_32x32x16_bf16 v[144:159], v[208:211], v[60:63], v[144:159]\n"
"ds_read_b64_tr_b16 v[204:205], v220 offset:4416\n"
"ds_read_b64_tr_b16 v[206:207], v220 offset:4544\n"
"v_mfma_f32_32x32x16_bf16 v[144:159], v[212:215], v[64:67], v[144:159]\n"
"s_waitcnt lgkmcnt(4)\n"
"ds_read_b64_tr_b16 v[208:209], v220 offset:4928\n"
"ds_read_b64_tr_b16 v[210:211], v220 offset:5056\n"
"v_mfma_f32_32x32x16_bf16 v[144:159], v[216:219], v[68:71], v[144:159]\n"
"ds_read_b64_tr_b16 v[212:213], v220 offset:8768\n"
"ds_read_b64_tr_b16 v[214:215], v220 offset:8896\n"
"v_mfma_f32_32x32x16_bf16 v[160:175], v[196:199], v[0:3], 0\n"
"s_waitcnt lgkmcnt(4)\n"
"ds_read_b64_tr_b16 v[216:217], v220 offset:9280\n"
"ds_read_b64_tr_b16 v[218:219], v220 offset:9408\n"
"v_mfma_f32_32x32x16_bf16 v[160:175], v[200:203], v[4:7], v[160:175]\n"
"ds_read_b64_tr_b16 v[196:197], v220 offset:13120\n"
"ds_read_b64_tr_b16 v[198:199], v220 offset:13248\n"
"v_mfma_f32_32x32x16_bf16 v[160:175], v[204:207], v[8:11], v[160:175]\n"
"s_waitcnt lgkmcnt(4)\n"
"ds_read_b64_tr_b16 v[200:201], v220 offset:13632\n"
"ds_read_b64_tr_b16 v[202:203], v220 offset:13760\n"
"v_mfma_f32_32x32x16_bf16 v[160:175], v[208:211], v[12:15], v[160:175]\n"
"ds_read_b64_tr_b16 v[204:205], v220 offset:17472\n"
"ds_read_b64_tr_b16 v[206:207], v220 offset:17600\n"
"v_mfma_f32_32x32x16_bf16 v[160:175], v[212:215], v[16:19], v[160:175]\n"
"s_waitcnt lgkmcnt(4)\n"
"ds_read_b64_tr_b16 v[208:209], v220 offset:17984\n"
"ds_read_b64_tr_b16 v[210:211], v220 offset:18112\n"
"v_mfma_f32_32x32x16_bf16 v[160:175], v[216:219], v[20:23], v[160:175]\n"
"ds_read_b64_tr_b16 v[212:213], v220 offset:21824\n"
"ds_read_b64_tr_b16 v[214:215], v220 offset:21952\n"
"v_mfma_f32_32x32x16_bf16 v[160:175], v[196:199], v[24:27], v[160:175]\n"
"s_waitcnt lgkmcnt(4)\n"
"ds_read_b64_tr_b16 v[216:217], v220 offset:22336\n"
"ds_read_b64_tr_b16 v[218:219], v220 offset:22464\n"
"v_mfma_f32_32x32x16_bf16 v[160:175], v[200:203], v[28:31], v[160:175]\n"
"ds_read_b64_tr_b16 v[196:197], v220 offset:26176\n"
"ds_read_b64_tr_b16 v[198:199], v220 offset:26304\n"
"v_mfma_f32_32x32x16_bf16 v[160:175], v[204:207], v[32:35], v[160:175]\n"
"s_waitcnt lgkmcnt(4)\n"
"ds_read_b64_tr_b16 v[200:201], v220 offset:26688\n"
"ds_read_b64_tr_b16 v[202:203], v220 offset:26816\n"
"v_mfma_f32_32x32x16_bf16 v[160:175], v[208:211], v[36:39], v[160:175]\n"
"ds_read_b64_tr_b16 v[204:205], v220 offset:30528\n"
"ds_read_b64_tr_b16 v[206:207], v220 offset:30656\n"
"v_mfma_f32_32x32x16_bf16 v[160:175], v[212:215], v[40:43], v[160:175]\n"
"s_waitcnt lgkmcnt(4)\n"
"ds_read_b64_tr_b16 v[208:209], v220 offset:31040\n"
"ds_read_b64_tr_b16 v[210:211], v220 offset:31168\n"
"v_mfma_f32_32x32x16_bf16 v[160:175], v[216:219], v[44:47], v[160:175]\n"
"ds_read_b64_tr_b16 v[212:213], v220 offset:34880\n"
"ds_read_b64_tr_b16 v[214:215], v220 offset:35008\n"
"v_mfma_f32_32x32x16_bf16 v[160:175], v[196:199], v[48:51], v[160:175]\n"
"s_waitcnt lgkmcnt(4)\n"
"ds_read_b64_tr_b16 v[216:217], v220 offset:35392\n"
"ds_read_b64_tr_b16 v[218:219], v220 offset:35520\n"
"v_add_u32 v220, s53, v221\n"
"v_mfma_f32_32x32x16_bf16 v[160:175], v[200:203], v[52:55], v[160:175]\n"
"s_waitcnt vmcnt(16)\n"
"s_barrier\n"
"ds_read_b64_tr_b16 v[196:197], v220 offset:0\n"
"ds_read_b64_tr_b16 v[198:199], v220 offset:128\n"
"v_mfma_f32_32x32x16_bf16 v[160:175], v[204:207], v[56:59], v[160:175]\n"
"s_waitcnt lgkmcnt(4)\n"
"ds_read_b64_tr_b16 v[200:201], v220 offset:512\n"
"ds_read_b64_tr_b16 v[202:203], v220 offset:640\n"
"v_mfma_f32_32x32x16_bf16 v[160:175], v[208:211], v[60:63], v[160:175]\n"
"s_add_u32 m0, s55, 30464\n"
"ds_read_b64_tr_b16 v[204:205], v220 offset:4352\n"
"ds_read_b64_tr_b16 v[206:207], v220 offset:4480\n"
"buffer_load_dwordx4 v222, s[44:47], s62 offen lds\n"
"v_mfma_f32_32x32x16_bf16 v[160:175], v[212:215], v[64:67], v[160:175]\n"
"s_waitcnt lgkmcnt(4)\n"
"ds_read_b64_tr_b16 v[208:209], v220 offset:4864\n"
"ds_read_b64_tr_b16 v[210:211], v220 offset:4992\n"
"v_mfma_f32_32x32x16_bf16 v[160:175], v[216:219], v[68:71], v[160:175]\n"
"s_add_u32 m0, s55, 34816\n"
"ds_read_b64_tr_b16 v[212:213], v220 offset:8704\n"
"ds_read_b64_tr_b16 v[214:215], v220 offset:8832\n"
"buffer_load_dwordx4 v222, s[44:47], s63 offen lds\n"
"v_mfma_f32_32x32x16_bf16 v[144:159], v[196:199], v[72:75], v[144:159]\n"
"s_waitcnt lgkmcnt(4)\n"
"ds_read_b64_tr_b16 v[216:217], v220 offset:9216\n"
"ds_read_b64_tr_b16 v[218:219], v220 offset:9344\n"
"v_mfma_f32_32x32x16_bf16 v[144:159], v[200:203], v[76:79], v[144:159]\n"
"s_add_u32 m0, s52, 0\n"
"ds_read_b64_tr_b16 v[196:197], v220 offset:13056\n"
"ds_read_b64_tr_b16 v[198:199], v220 offset:13184\n"
"buffer_load_dwordx4 v222, s[44:47], s64 offen lds\n"
"v_mfma_f32_32x32x16_bf16 v[144:159], v[204:207], v[80:83], v[144:159]\n"
"s_waitcnt lgkmcnt(4)\n"
"ds_read_b64_tr_b16 v[200:201], v220 offset:13568\n"
"ds_read_b64_tr_b16 v[202:203], v220 offset:13696\n"
"v_mfma_f32_32x32x16_bf16 v[144:159], v[208:211], v[84:87], v[144:159]\n"
"s_add_u32 m0, s52, 4352\n"
"ds_read_b64_tr_b16 v[204:205], v220 offset:17408\n"
"ds_read_b64_tr_b16 v[206:207], v220 offset:17536\n"
"buffer_load_dwordx4 v222, s[44:47], s65 offen lds\n"
"v_mfma_f32_32x32x16_bf16 v[144:159], v[212:215], v[88:91], v[144:159]\n"
"s_waitcnt lgkmcnt(4)\n"
"ds_read_b64_tr_b16 v[208:209], v220 offset:17920\n"
"ds_read_b64_tr_b16 v[210:211], v220 offset:18048\n"
"v_mfma_f32_32x32x16_bf16 v[144:159], v[216:219], v[92:95], v[144:159]\n"
"s_add_u32 m0, s52, 8704\n"
"ds_read_b64_tr_b16 v[212:213], v220 offset:21760\n"
"ds_read_b64_tr_b16 v[214:215], v220 offset:21888\n"
"buffer_load_dwordx4 v222, s[44:47], s66 offen lds\n"
"v_mfma_f32_32x32x16_bf16 v[144:159], v[196:199], v[96:99], v[144:159]\n"
"s_waitcnt lgkmcnt(4)\n"
"ds_read_b64_tr_b16 v[216:217], v220 offset:22272\n"
"ds_read_b64_tr_b16 v[218:219], v220 offset:22400\n"
"v_mfma_f32_32x32x16_bf16 v[144:159], v[200:203], v[100:103], v[144:159]\n"
"s_add_u32 m0, s52, 13056\n"
"ds_read_b64_tr_b16 v[196:197], v220 offset:26112\n"
"ds_read_b64_tr_b16 v[198:199], v220 offset:26240\n"
"buffer_load_dwordx4 v222, s[44:47], s67 offen lds\n"
"v_mfma_f32_32x32x16_bf16 v[144:159], v[204:207], v[104:107], v[144:159]\n"
"s_waitcnt lgkmcnt(4)\n"
"ds_read_b64_tr_b16 v[200:201], v220 offset:26624\n"
"ds_read_b64_tr_b16 v[202:203], v220 offset:26752\n"
"v_mfma_f32_32x32x16_bf16 v[144:159], v[208:211], v[108:111], v[144:159]\n"
"s_add_u32 m0, s52, 17408\n"
"ds_read_b64_tr_b16 v[204:205], v220 offset:30464\n"
"ds_read_b64_tr_b16 v[206:207], v220 offset:30592\n"
"buffer_load_dwordx4 v222, s[44:47], s68 offen lds\n"
"v_mfma_f32_32x32x16_bf16 v[144:159], v[212:215], v[112:115], v[144:159]\n"
"s_waitcnt lgkmcnt(4)\n"
"ds_read_b64_tr_b16 v[208:209], v220 offset:30976\n"
"ds_read_b64_tr_b16 v[210:211], v220 offset:31104\n"
"v_mfma_f32_32x32x16_bf16 v[144:159], v[216:219], v[116:119], v[144:159]\n"
"s_add_u32 m0, s52, 21760\n"
"ds_read_b64_tr_b16 v[212:213], v220 offset:34816\n"
"ds_read_b64_tr_b16 v[214:215], v220 offset:34944\n"
"buffer_load_dwordx4 v222, s[44:47], s69 offen lds\n"
"v_mfma_f32_32x32x16_bf16 v[144:159], v[196:199], v[120:123], v[144:159]\n"
"s_waitcnt lgkmcnt(4)\n"
"ds_read_b64_tr_b16 v[216:217], v220 offset:35328\n"
"ds_read_b64_tr_b16 v[218:219], v220 offset:35456\n"
"v_mfma_f32_32x32x16_bf16 v[144:159], v[200:203], v[124:127], v[144:159]\n"
"s_add_u32 m0, s52, 26112\n"
"ds_read_b64_tr_b16 v[196:197], v220 offset:64\n"
"ds_read_b64_tr_b16 v[198:199], v220 offset:192\n"
"buffer_load_dwordx4 v222, s[44:47], s70 offen lds\n"
"v_mfma_f32_32x32x16_bf16 v[144:159], v[204:207], v[128:131], v[144:159]\n"
"s_waitcnt lgkmcnt(4)\n"
"ds_read_b64_tr_b16 v[200:201], v220 offset:576\n"
"ds_read_b64_tr_b16 v[202:203], v220 offset:704\n"
"v_mfma_f32_32x32x16_bf16 v[144:159], v[208:211], v[132:135], v[144:159]\n"
"ds_read_b64_tr_b16 v[204:205], v220 offset:4416\n"
"ds_read_b64_tr_b16 v[206:207], v220 offset:4544\n"
"v_mfma_f32_32x32x16_bf16 v[144:159], v[212:215], v[136:139], v[144:159]\n"
"s_waitcnt lgkmcnt(4)\n"
"ds_read_b64_tr_b16 v[208:209], v220 offset:4928\n"
"ds_read_b64_tr_b16 v[210:211], v220 offset:5056\n"
"v_mfma_f32_32x32x16_bf16 v[144:159], v[216:219], v[140:143], v[144:159]\n"
"ds_read_b64_tr_b16 v[212:213], v220 offset:8768\n"
"ds_read_b64_tr_b16 v[214:215], v220 offset:8896\n"
"v_mfma_f32_32x32x16_bf16 v[160:175], v[196:199], v[72:75], v[160:175]\n"
"s_waitcnt lgkmcnt(4)\n"
"ds_read_b64_tr_b16 v[216:217], v220 offset:9280\n"
"ds_read_b64_tr_b16 v[218:219], v220 offset:9408\n"
"v_mfma_f32_32x32x16_bf16 v[160:175], v[200:203], v[76:79], v[160:175]\n"
"ds_read_b64_tr_b16 v[196:197], v220 offset:13120\n"
"ds_read_b64_tr_b16 v[198:199], v220 offset:13248\n"
"v_mfma_f32_32x32x16_bf16 v[160:175], v[204:207], v[80:83], v[160:175]\n"
"s_waitcnt lgkmcnt(4)\n"
"ds_read_b64_tr_b16 v[200:201], v220 offset:13632\n"
"ds_read_b64_tr_b16 v[202:203], v220 offset:13760\n"
"v_exp_f32 v192, -v144\n"
"v_mfma_f32_32x32x16_bf16 v[160:175], v[208:211], v[84:87], v[160:175]\n"
"ds_read_b64_tr_b16 v[204:205], v220 offset:17472\n"
"ds_read_b64_tr_b16 v[206:207], v220 offset:17600\n"
"v_add_f32 v192, 1.0, v192\n"
"v_rcp_f32 v192, v192\n"
"v_mfma_f32_32x32x16_bf16 v[160:175], v[212:215], v[88:91], v[160:175]\n"
"s_waitcnt lgkmcnt(4)\n"
"ds_read_b64_tr_b16 v[208:209], v220 offset:17984\n"
"ds_read_b64_tr_b16 v[210:211], v220 offset:18112\n"
"v_mul_f32 v144, v144, v192\n"
"v_exp_f32 v193, -v145\n"
"v_mfma_f32_32x32x16_bf16 v[160:175], v[216:219], v[92:95], v[160:175]\n"
"ds_read_b64_tr_b16 v[212:213], v220 offset:21824\n"
"ds_read_b64_tr_b16 v[214:215], v220 offset:21952\n"
"v_add_f32 v193, 1.0, v193\n"
"v_rcp_f32 v193, v193\n"
"v_mfma_f32_32x32x16_bf16 v[160:175], v[196:199], v[96:99], v[160:175]\n"
"s_waitcnt lgkmcnt(4)\n"
"ds_read_b64_tr_b16 v[216:217], v220 offset:22336\n"
"ds_read_b64_tr_b16 v[218:219], v220 offset:22464\n"
"v_mul_f32 v145, v145, v193\n"
"v_exp_f32 v194, -v146\n"
"v_mfma_f32_32x32x16_bf16 v[160:175], v[200:203], v[100:103], v[160:175]\n"
"ds_read_b64_tr_b16 v[196:197], v220 offset:26176\n"
"ds_read_b64_tr_b16 v[198:199], v220 offset:26304\n"
"v_add_f32 v194, 1.0, v194\n"
"v_rcp_f32 v194, v194\n"
"v_mfma_f32_32x32x16_bf16 v[160:175], v[204:207], v[104:107], v[160:175]\n"
"s_waitcnt lgkmcnt(4)\n"
"ds_read_b64_tr_b16 v[200:201], v220 offset:26688\n"
"ds_read_b64_tr_b16 v[202:203], v220 offset:26816\n"
"v_mul_f32 v146, v146, v194\n"
"v_exp_f32 v195, -v147\n"
"v_mfma_f32_32x32x16_bf16 v[160:175], v[208:211], v[108:111], v[160:175]\n"
"ds_read_b64_tr_b16 v[204:205], v220 offset:30528\n"
"ds_read_b64_tr_b16 v[206:207], v220 offset:30656\n"
"v_add_f32 v195, 1.0, v195\n"
"v_rcp_f32 v195, v195\n"
"v_mfma_f32_32x32x16_bf16 v[160:175], v[212:215], v[112:115], v[160:175]\n"
"s_waitcnt lgkmcnt(4)\n"
"ds_read_b64_tr_b16 v[208:209], v220 offset:31040\n"
"ds_read_b64_tr_b16 v[210:211], v220 offset:31168\n"
"v_mul_f32 v147, v147, v195\n"
"v_exp_f32 v192, -v148\n"
"v_mfma_f32_32x32x16_bf16 v[160:175], v[216:219], v[116:119], v[160:175]\n"
"ds_read_b64_tr_b16 v[212:213], v220 offset:34880\n"
"ds_read_b64_tr_b16 v[214:215], v220 offset:35008\n"
"v_add_f32 v192, 1.0, v192\n"
"v_rcp_f32 v192, v192\n"
"v_mfma_f32_32x32x16_bf16 v[160:175], v[196:199], v[120:123], v[160:175]\n"
"s_waitcnt lgkmcnt(4)\n"
"ds_read_b64_tr_b16 v[216:217], v220 offset:35392\n"
"ds_read_b64_tr_b16 v[218:219], v220 offset:35520\n"
"v_add_u32 v220, s54, v221\n"
"v_mul_f32 v148, v148, v192\n"
"v_mfma_f32_32x32x16_bf16 v[160:175], v[200:203], v[124:127], v[160:175]\n"
"s_waitcnt vmcnt(16)\n"
"s_barrier\n"
"ds_read_b64_tr_b16 v[196:197], v220 offset:0\n"
"ds_read_b64_tr_b16 v[198:199], v220 offset:128\n"
"v_cvt_pk_bf16_f32 v176, v144, v145\n"
"v_mfma_f32_32x32x16_bf16 v[160:175], v[204:207], v[128:131], v[160:175]\n"
"s_waitcnt lgkmcnt(4)\n"
"ds_read_b64_tr_b16 v[200:201], v220 offset:64\n"
"ds_read_b64_tr_b16 v[202:203], v220 offset:192\n"
"v_cvt_pk_bf16_f32 v177, v146, v147\n"
"v_exp_f32 v193, -v149\n"
"v_mfma_f32_32x32x16_bf16 v[160:175], v[208:211], v[132:135], v[160:175]\n"
"s_add_u32 m0, s52, 30464\n"
"ds_read_b64_tr_b16 v[204:205], v220 offset:4352\n"
"ds_read_b64_tr_b16 v[206:207], v220 offset:4480\n"
"buffer_load_dwordx4 v222, s[44:47], s71 offen lds\n"
"v_exp_f32 v194, -v150\n"
"v_mfma_f32_32x32x16_bf16 v[160:175], v[212:215], v[136:139], v[160:175]\n"
"s_waitcnt lgkmcnt(4)\n"
"ds_read_b64_tr_b16 v[208:209], v220 offset:4416\n"
"ds_read_b64_tr_b16 v[210:211], v220 offset:4544\n"
"v_add_f32 v193, 1.0, v193\n"
"v_rcp_f32 v193, v193\n"
"v_mfma_f32_32x32x16_bf16 v[160:175], v[216:219], v[140:143], v[160:175]\n"
"s_add_u32 m0, s52, 34816\n"
"ds_read_b64_tr_b16 v[212:213], v220 offset:8704\n"
"ds_read_b64_tr_b16 v[214:215], v220 offset:8832\n"
"buffer_load_dwordx4 v222, s[44:47], s72 offen lds\n"
"v_add_u32 v222, 128, v222\n"
"v_mul_f32 v149, v149, v193\n"
"v_mfma_f32_32x32x16_bf16 a[0:15], v[196:199], v[184:187], a[0:15]\n"
"s_waitcnt lgkmcnt(4)\n"
"ds_read_b64_tr_b16 v[216:217], v220 offset:8768\n"
"ds_read_b64_tr_b16 v[218:219], v220 offset:8896\n"
"v_add_f32 v194, 1.0, v194\n"
"v_rcp_f32 v194, v194\n"
"v_mfma_f32_32x32x16_bf16 a[16:31], v[200:203], v[184:187], a[16:31]\n"
"ds_read_b64_tr_b16 v[196:197], v220 offset:13056\n"
"ds_read_b64_tr_b16 v[198:199], v220 offset:13184\n"
"v_mul_f32 v150, v150, v194\n"
"v_exp_f32 v195, -v151\n"
"v_mfma_f32_32x32x16_bf16 a[32:47], v[204:207], v[184:187], a[32:47]\n"
"s_waitcnt lgkmcnt(4)\n"
"ds_read_b64_tr_b16 v[200:201], v220 offset:13120\n"
"ds_read_b64_tr_b16 v[202:203], v220 offset:13248\n"
"v_add_f32 v195, 1.0, v195\n"
"v_rcp_f32 v195, v195\n"
"v_mfma_f32_32x32x16_bf16 a[48:63], v[208:211], v[184:187], a[48:63]\n"
"s_add_u32 m0, s53, 0\n"
"ds_read_b64_tr_b16 v[204:205], v220 offset:17408\n"
"ds_read_b64_tr_b16 v[206:207], v220 offset:17536\n"
"buffer_load_dwordx4 v223, s[48:51], 0 offen lds\n"
"v_exp_f32 v192, -v152\n"
"v_mfma_f32_32x32x16_bf16 a[64:79], v[212:215], v[184:187], a[64:79]\n"
"s_waitcnt lgkmcnt(4)\n"
"ds_read_b64_tr_b16 v[208:209], v220 offset:17472\n"
"ds_read_b64_tr_b16 v[210:211], v220 offset:17600\n"
"v_mul_f32 v151, v151, v195\n"
"v_exp_f32 v193, -v153\n"
"v_mfma_f32_32x32x16_bf16 a[80:95], v[216:219], v[184:187], a[80:95]\n"
"ds_read_b64_tr_b16 v[212:213], v220 offset:21760\n"
"ds_read_b64_tr_b16 v[214:215], v220 offset:21888\n"
"v_add_f32 v192, 1.0, v192\n"
"v_rcp_f32 v192, v192\n"
"v_mfma_f32_32x32x16_bf16 a[96:111], v[196:199], v[184:187], a[96:111]\n"
"s_waitcnt lgkmcnt(4)\n"
"ds_read_b64_tr_b16 v[216:217], v220 offset:21824\n"
"ds_read_b64_tr_b16 v[218:219], v220 offset:21952\n"
"v_mul_f32 v152, v152, v192\n"
"v_exp_f32 v194, -v154\n"
"v_mfma_f32_32x32x16_bf16 a[112:127], v[200:203], v[184:187], a[112:127]\n"
"s_add_u32 m0, s53, 4224\n"
"ds_read_b64_tr_b16 v[196:197], v220 offset:26112\n"
"ds_read_b64_tr_b16 v[198:199], v220 offset:26240\n"
"buffer_load_dwordx4 v223, s[48:51], 0 offen offset:128 lds\n"
"v_exp_f32 v195, -v155\n"
"v_mfma_f32_32x32x16_bf16 a[128:143], v[204:207], v[184:187], a[128:143]\n"
"s_waitcnt lgkmcnt(4)\n"
"ds_read_b64_tr_b16 v[200:201], v220 offset:26176\n"
"ds_read_b64_tr_b16 v[202:203], v220 offset:26304\n"
"v_add_f32 v193, 1.0, v193\n"
"v_rcp_f32 v193, v193\n"
"v_mfma_f32_32x32x16_bf16 a[144:159], v[208:211], v[184:187], a[144:159]\n"
"ds_read_b64_tr_b16 v[204:205], v220 offset:30464\n"
"ds_read_b64_tr_b16 v[206:207], v220 offset:30592\n"
"v_add_f32 v194, 1.0, v194\n"
"v_rcp_f32 v194, v194\n"
"v_mfma_f32_32x32x16_bf16 a[160:175], v[212:215], v[184:187], a[160:175]\n"
"s_waitcnt lgkmcnt(4)\n"
"ds_read_b64_tr_b16 v[208:209], v220 offset:30528\n"
"ds_read_b64_tr_b16 v[210:211], v220 offset:30656\n"
"v_mul_f32 v153, v153, v193\n"
"v_exp_f32 v192, -v156\n"
"v_mfma_f32_32x32x16_bf16 a[176:191], v[216:219], v[184:187], a[176:191]\n"
"s_add_u32 m0, s53, 8448\n"
"ds_read_b64_tr_b16 v[212:213], v220 offset:34816\n"
"ds_read_b64_tr_b16 v[214:215], v220 offset:34944\n"
"buffer_load_dwordx4 v223, s[48:51], 0 offen offset:256 lds\n"
"v_exp_f32 v193, -v157\n"
"v_mfma_f32_32x32x16_bf16 a[192:207], v[196:199], v[184:187], a[192:207]\n"
"s_waitcnt lgkmcnt(4)\n"
"ds_read_b64_tr_b16 v[216:217], v220 offset:34880\n"
"ds_read_b64_tr_b16 v[218:219], v220 offset:35008\n"
"v_add_f32 v195, 1.0, v195\n"
"v_rcp_f32 v195, v195\n"
"v_mfma_f32_32x32x16_bf16 a[208:223], v[200:203], v[184:187], a[208:223]\n"
"ds_read_b64_tr_b16 v[196:197], v220 offset:512\n"
"ds_read_b64_tr_b16 v[198:199], v220 offset:640\n"
"v_mul_f32 v154, v154, v194\n"
"v_exp_f32 v194, -v158\n"
"v_mfma_f32_32x32x16_bf16 a[224:239], v[204:207], v[184:187], a[224:239]\n"
"s_waitcnt lgkmcnt(4)\n"
"ds_read_b64_tr_b16 v[200:201], v220 offset:576\n"
"ds_read_b64_tr_b16 v[202:203], v220 offset:704\n"
"v_add_f32 v192, 1.0, v192\n"
"v_rcp_f32 v192, v192\n"
"v_mfma_f32_32x32x16_bf16 a[240:255], v[208:211], v[184:187], a[240:255]\n"
"s_add_u32 m0, s53, 12672\n"
"ds_read_b64_tr_b16 v[204:205], v220 offset:4864\n"
"ds_read_b64_tr_b16 v[206:207], v220 offset:4992\n"
"buffer_load_dwordx4 v223, s[48:51], 0 offen offset:384 lds\n"
"v_mul_f32 v155, v155, v195\n"
"v_mul_f32 v156, v156, v192\n"
"v_mfma_f32_32x32x16_bf16 v[224:239], v[212:215], v[184:187], v[224:239]\n"
"s_waitcnt lgkmcnt(4)\n"
"ds_read_b64_tr_b16 v[208:209], v220 offset:4928\n"
"ds_read_b64_tr_b16 v[210:211], v220 offset:5056\n"
"v_add_f32 v193, 1.0, v193\n"
"v_rcp_f32 v193, v193\n"
"v_mfma_f32_32x32x16_bf16 v[240:255], v[216:219], v[184:187], v[240:255]\n"
"ds_read_b64_tr_b16 v[212:213], v220 offset:9216\n"
"ds_read_b64_tr_b16 v[214:215], v220 offset:9344\n"
"v_mul_f32 v157, v157, v193\n"
"v_exp_f32 v195, -v159\n"
"v_mfma_f32_32x32x16_bf16 a[0:15], v[196:199], v[188:191], a[0:15]\n"
"s_waitcnt lgkmcnt(4)\n"
"ds_read_b64_tr_b16 v[216:217], v220 offset:9280\n"
"ds_read_b64_tr_b16 v[218:219], v220 offset:9408\n"
"v_add_f32 v194, 1.0, v194\n"
"v_rcp_f32 v194, v194\n"
"v_mfma_f32_32x32x16_bf16 a[16:31], v[200:203], v[188:191], a[16:31]\n"
"s_add_u32 m0, s53, 16896\n"
"ds_read_b64_tr_b16 v[196:197], v220 offset:13568\n"
"ds_read_b64_tr_b16 v[198:199], v220 offset:13696\n"
"buffer_load_dwordx4 v223, s[48:51], 0 offen offset:512 lds\n"
"v_exp_f32 v192, -v160\n"
"v_mfma_f32_32x32x16_bf16 a[32:47], v[204:207], v[188:191], a[32:47]\n"
"s_waitcnt lgkmcnt(4)\n"
"ds_read_b64_tr_b16 v[200:201], v220 offset:13632\n"
"ds_read_b64_tr_b16 v[202:203], v220 offset:13760\n"
"v_mul_f32 v158, v158, v194\n"
"v_exp_f32 v193, -v161\n"
"v_mfma_f32_32x32x16_bf16 a[48:63], v[208:211], v[188:191], a[48:63]\n"
"ds_read_b64_tr_b16 v[204:205], v220 offset:17920\n"
"ds_read_b64_tr_b16 v[206:207], v220 offset:18048\n"
"v_add_f32 v195, 1.0, v195\n"
"v_rcp_f32 v195, v195\n"
"v_mfma_f32_32x32x16_bf16 a[64:79], v[212:215], v[188:191], a[64:79]\n"
"s_waitcnt lgkmcnt(4)\n"
"ds_read_b64_tr_b16 v[208:209], v220 offset:17984\n"
"ds_read_b64_tr_b16 v[210:211], v220 offset:18112\n"
"v_add_f32 v192, 1.0, v192\n"
"v_rcp_f32 v192, v192\n"
"v_mfma_f32_32x32x16_bf16 a[80:95], v[216:219], v[188:191], a[80:95]\n"
"s_add_u32 m0, s53, 21120\n"
"ds_read_b64_tr_b16 v[212:213], v220 offset:22272\n"
"ds_read_b64_tr_b16 v[214:215], v220 offset:22400\n"
"buffer_load_dwordx4 v223, s[48:51], 0 offen offset:640 lds\n"
"v_mul_f32 v159, v159, v195\n"
"v_mul_f32 v160, v160, v192\n"
"v_mfma_f32_32x32x16_bf16 a[96:111], v[196:199], v[188:191], a[96:111]\n"
"s_waitcnt lgkmcnt(4)\n"
"ds_read_b64_tr_b16 v[216:217], v220 offset:22336\n"
"ds_read_b64_tr_b16 v[218:219], v220 offset:22464\n"
"v_add_f32 v193, 1.0, v193\n"
"v_rcp_f32 v193, v193\n"
"v_mfma_f32_32x32x16_bf16 a[112:127], v[200:203], v[188:191], a[112:127]\n"
"ds_read_b64_tr_b16 v[196:197], v220 offset:26624\n"
"ds_read_b64_tr_b16 v[198:199], v220 offset:26752\n"
"v_mul_f32 v161, v161, v193\n"
"v_exp_f32 v194, -v162\n"
"v_mfma_f32_32x32x16_bf16 a[128:143], v[204:207], v[188:191], a[128:143]\n"
"s_waitcnt lgkmcnt(4)\n"
"ds_read_b64_tr_b16 v[200:201], v220 offset:26688\n"
"ds_read_b64_tr_b16 v[202:203], v220 offset:26816\n"
"v_add_f32 v194, 1.0, v194\n"
"v_rcp_f32 v194, v194\n"
"v_mfma_f32_32x32x16_bf16 a[144:159], v[208:211], v[188:191], a[144:159]\n"
"s_add_u32 m0, s53, 25344\n"
"ds_read_b64_tr_b16 v[204:205], v220 offset:30976\n"
"ds_read_b64_tr_b16 v[206:207], v220 offset:31104\n"
"buffer_load_dwordx4 v223, s[48:51], 0 offen offset:768 lds\n"
"v_cvt_pk_bf16_f32 v178, v148, v149\n"
"v_mul_f32 v162, v162, v194\n"
"v_mfma_f32_32x32x16_bf16 a[160:175], v[212:215], v[188:191], a[160:175]\n"
"s_waitcnt lgkmcnt(4)\n"
"ds_read_b64_tr_b16 v[208:209], v220 offset:31040\n"
"ds_read_b64_tr_b16 v[210:211], v220 offset:31168\n"
"v_cvt_pk_bf16_f32 v179, v150, v151\n"
"v_exp_f32 v195, -v163\n"
"v_mfma_f32_32x32x16_bf16 a[176:191], v[216:219], v[188:191], a[176:191]\n"
"ds_read_b64_tr_b16 v[212:213], v220 offset:35328\n"
"ds_read_b64_tr_b16 v[214:215], v220 offset:35456\n"
"v_cvt_pk_bf16_f32 v180, v152, v153\n"
"v_exp_f32 v192, -v164\n"
"v_mfma_f32_32x32x16_bf16 a[192:207], v[196:199], v[188:191], a[192:207]\n"
"s_waitcnt lgkmcnt(4)\n"
"ds_read_b64_tr_b16 v[216:217], v220 offset:35392\n"
"ds_read_b64_tr_b16 v[218:219], v220 offset:35520\n"
"v_add_u32 v220, s55, v221\n"
"v_cvt_pk_bf16_f32 v181, v154, v155\n"
"v_mfma_f32_32x32x16_bf16 a[208:223], v[200:203], v[188:191], a[208:223]\n"
"s_waitcnt vmcnt(16)\n"
"s_barrier\n"
"ds_read_b64_tr_b16 v[196:197], v220 offset:0\n"
"ds_read_b64_tr_b16 v[198:199], v220 offset:128\n"
"v_cvt_pk_bf16_f32 v182, v156, v157\n"
"v_mfma_f32_32x32x16_bf16 a[224:239], v[204:207], v[188:191], a[224:239]\n"
"s_waitcnt lgkmcnt(4)\n"
"ds_read_b64_tr_b16 v[200:201], v220 offset:512\n"
"ds_read_b64_tr_b16 v[202:203], v220 offset:640\n"
"v_cvt_pk_bf16_f32 v183, v158, v159\n"
"v_exp_f32 v193, -v165\n"
"v_mfma_f32_32x32x16_bf16 a[240:255], v[208:211], v[188:191], a[240:255]\n"
"s_add_u32 m0, s53, 29568\n"
"ds_read_b64_tr_b16 v[204:205], v220 offset:4352\n"
"ds_read_b64_tr_b16 v[206:207], v220 offset:4480\n"
"buffer_load_dwordx4 v223, s[48:51], 0 offen offset:896 lds\n"
"v_exp_f32 v194, -v166\n"
"v_mfma_f32_32x32x16_bf16 v[224:239], v[212:215], v[188:191], v[224:239]\n"
"s_waitcnt lgkmcnt(4)\n"
"ds_read_b64_tr_b16 v[208:209], v220 offset:4864\n"
"ds_read_b64_tr_b16 v[210:211], v220 offset:4992\n"
"v_add_f32 v195, 1.0, v195\n"
"v_rcp_f32 v195, v195\n"
"v_mfma_f32_32x32x16_bf16 v[240:255], v[216:219], v[188:191], v[240:255]\n"
"ds_read_b64_tr_b16 v[212:213], v220 offset:8704\n"
"ds_read_b64_tr_b16 v[214:215], v220 offset:8832\n"
"v_add_f32 v192, 1.0, v192\n"
"v_rcp_f32 v192, v192\n"
"v_mfma_f32_32x32x16_bf16 v[144:159], v[196:199], v[0:3], 0\n"
"s_waitcnt lgkmcnt(4)\n"
"ds_read_b64_tr_b16 v[216:217], v220 offset:9216\n"
"ds_read_b64_tr_b16 v[218:219], v220 offset:9344\n"
"v_mul_f32 v163, v163, v195\n"
"v_exp_f32 v195, -v167\n"
"v_mfma_f32_32x32x16_bf16 v[144:159], v[200:203], v[4:7], v[144:159]\n"
"s_add_u32 m0, s53, 33792\n"
"ds_read_b64_tr_b16 v[196:197], v220 offset:13056\n"
"ds_read_b64_tr_b16 v[198:199], v220 offset:13184\n"
"buffer_load_dwordx4 v223, s[48:51], 0 offen offset:1024 lds\n"
"v_add_u32 v223, s77, v223\n"
"v_mul_f32 v164, v164, v192\n"
"v_mfma_f32_32x32x16_bf16 v[144:159], v[204:207], v[8:11], v[144:159]\n"
"s_waitcnt lgkmcnt(4)\n"
"ds_read_b64_tr_b16 v[200:201], v220 offset:13568\n"
"ds_read_b64_tr_b16 v[202:203], v220 offset:13696\n"
"v_add_f32 v193, 1.0, v193\n"
"v_rcp_f32 v193, v193\n"
"v_mfma_f32_32x32x16_bf16 v[144:159], v[208:211], v[12:15], v[144:159]\n"
"ds_read_b64_tr_b16 v[204:205], v220 offset:17408\n"
"ds_read_b64_tr_b16 v[206:207], v220 offset:17536\n"
"v_add_f32 v194, 1.0, v194\n"
"v_rcp_f32 v194, v194\n"
"v_mfma_f32_32x32x16_bf16 v[144:159], v[212:215], v[16:19], v[144:159]\n"
"s_waitcnt lgkmcnt(4)\n"
"ds_read_b64_tr_b16 v[208:209], v220 offset:17920\n"
"ds_read_b64_tr_b16 v[210:211], v220 offset:18048\n"
"v_mul_f32 v165, v165, v193\n"
"v_exp_f32 v192, -v168\n"
"v_mfma_f32_32x32x16_bf16 v[144:159], v[216:219], v[20:23], v[144:159]\n"
"s_add_u32 m0, s54, 0\n"
"ds_read_b64_tr_b16 v[212:213], v220 offset:21760\n"
"ds_read_b64_tr_b16 v[214:215], v220 offset:21888\n"
"buffer_load_dwordx4 v222, s[40:43], 0 offen lds\n"
"v_exp_f32 v193, -v169\n"
"v_mfma_f32_32x32x16_bf16 v[144:159], v[196:199], v[24:27], v[144:159]\n"
"s_waitcnt lgkmcnt(4)\n"
"ds_read_b64_tr_b16 v[216:217], v220 offset:22272\n"
"ds_read_b64_tr_b16 v[218:219], v220 offset:22400\n"
"v_add_f32 v195, 1.0, v195\n"
"v_rcp_f32 v195, v195\n"
"v_mfma_f32_32x32x16_bf16 v[144:159], v[200:203], v[28:31], v[144:159]\n"
"ds_read_b64_tr_b16 v[196:197], v220 offset:26112\n"
"ds_read_b64_tr_b16 v[198:199], v220 offset:26240\n"
"v_mul_f32 v166, v166, v194\n"
"v_exp_f32 v194, -v170\n"
"v_mfma_f32_32x32x16_bf16 v[144:159], v[204:207], v[32:35], v[144:159]\n"
"s_waitcnt lgkmcnt(4)\n"
"ds_read_b64_tr_b16 v[200:201], v220 offset:26624\n"
"ds_read_b64_tr_b16 v[202:203], v220 offset:26752\n"
"v_add_f32 v192, 1.0, v192\n"
"v_rcp_f32 v192, v192\n"
"v_mfma_f32_32x32x16_bf16 v[144:159], v[208:211], v[36:39], v[144:159]\n"
"s_add_u32 m0, s54, 4352\n"
"ds_read_b64_tr_b16 v[204:205], v220 offset:30464\n"
"ds_read_b64_tr_b16 v[206:207], v220 offset:30592\n"
"buffer_load_dwordx4 v222, s[40:43], s56 offen lds\n"
"v_mul_f32 v167, v167, v195\n"
"v_mul_f32 v168, v168, v192\n"
"v_mfma_f32_32x32x16_bf16 v[144:159], v[212:215], v[40:43], v[144:159]\n"
"s_waitcnt lgkmcnt(4)\n"
"ds_read_b64_tr_b16 v[208:209], v220 offset:30976\n"
"ds_read_b64_tr_b16 v[210:211], v220 offset:31104\n"
"v_add_f32 v193, 1.0, v193\n"
"v_rcp_f32 v193, v193\n"
"v_mfma_f32_32x32x16_bf16 v[144:159], v[216:219], v[44:47], v[144:159]\n"
"ds_read_b64_tr_b16 v[212:213], v220 offset:34816\n"
"ds_read_b64_tr_b16 v[214:215], v220 offset:34944\n"
"v_mul_f32 v169, v169, v193\n"
"v_exp_f32 v195, -v171\n"
"v_mfma_f32_32x32x16_bf16 v[144:159], v[196:199], v[48:51], v[144:159]\n"
"s_waitcnt lgkmcnt(4)\n"
"ds_read_b64_tr_b16 v[216:217], v220 offset:35328\n"
"ds_read_b64_tr_b16 v[218:219], v220 offset:35456\n"
"v_add_u32 v220, s52, v221\n"
"v_add_f32 v194, 1.0, v194\n"
"v_mfma_f32_32x32x16_bf16 v[144:159], v[200:203], v[52:55], v[144:159]\n"
"s_waitcnt vmcnt(11)\n"
"s_barrier\n"
"ds_read_b64_tr_b16 v[196:197], v220 offset:0\n"
"ds_read_b64_tr_b16 v[198:199], v220 offset:128\n"
"v_add_f32 v195, 1.0, v195\n"
"v_mfma_f32_32x32x16_bf16 v[144:159], v[204:207], v[56:59], v[144:159]\n"
"s_waitcnt lgkmcnt(4)\n"
"ds_read_b64_tr_b16 v[200:201], v220 offset:512\n"
"ds_read_b64_tr_b16 v[202:203], v220 offset:640\n"
"v_cvt_pk_bf16_f32 v184, v160, v161\n"
"v_rcp_f32 v194, v194\n"
"v_mfma_f32_32x32x16_bf16 v[144:159], v[208:211], v[60:63], v[144:159]\n"
"s_add_u32 m0, s54, 8704\n"
"ds_read_b64_tr_b16 v[204:205], v220 offset:4352\n"
"ds_read_b64_tr_b16 v[206:207], v220 offset:4480\n"
"buffer_load_dwordx4 v222, s[40:43], s57 offen lds\n"
"v_rcp_f32 v195, v195\n"
"v_mfma_f32_32x32x16_bf16 v[144:159], v[212:215], v[64:67], v[144:159]\n"
"s_waitcnt lgkmcnt(4)\n"
"ds_read_b64_tr_b16 v[208:209], v220 offset:4864\n"
"ds_read_b64_tr_b16 v[210:211], v220 offset:4992\n"
"v_mul_f32 v170, v170, v194\n"
"v_exp_f32 v192, -v172\n"
"v_mfma_f32_32x32x16_bf16 v[144:159], v[216:219], v[68:71], v[144:159]\n"
"s_add_u32 m0, s54, 13056\n"
"ds_read_b64_tr_b16 v[212:213], v220 offset:8704\n"
"ds_read_b64_tr_b16 v[214:215], v220 offset:8832\n"
"buffer_load_dwordx4 v222, s[40:43], s58 offen lds\n"
"v_exp_f32 v193, -v173\n"
"v_mfma_f32_32x32x16_bf16 v[144:159], v[196:199], v[72:75], v[144:159]\n"
"s_waitcnt lgkmcnt(4)\n"
"ds_read_b64_tr_b16 v[216:217], v220 offset:9216\n"
"ds_read_b64_tr_b16 v[218:219], v220 offset:9344\n"
"v_add_f32 v192, 1.0, v192\n"
"v_rcp_f32 v192, v192\n"
"v_mfma_f32_32x32x16_bf16 v[144:159], v[200:203], v[76:79], v[144:159]\n"
"ds_read_b64_tr_b16 v[196:197], v220 offset:13056\n"
"ds_read_b64_tr_b16 v[198:199], v220 offset:13184\n"
"v_mul_f32 v171, v171, v195\n"
"v_exp_f32 v194, -v174\n"
"v_mfma_f32_32x32x16_bf16 v[144:159], v[204:207], v[80:83], v[144:159]\n"
"s_waitcnt lgkmcnt(4)\n"
"ds_read_b64_tr_b16 v[200:201], v220 offset:13568\n"
"ds_read_b64_tr_b16 v[202:203], v220 offset:13696\n"
"v_mul_f32 v172, v172, v192\n"
"v_exp_f32 v195, -v175\n"
"v_mfma_f32_32x32x16_bf16 v[144:159], v[208:211], v[84:87], v[144:159]\n"
"s_add_u32 m0, s54, 17408\n"
"ds_read_b64_tr_b16 v[204:205], v220 offset:17408\n"
"ds_read_b64_tr_b16 v[206:207], v220 offset:17536\n"
"buffer_load_dwordx4 v222, s[40:43], s59 offen lds\n"
"v_add_f32 v193, 1.0, v193\n"
"v_add_f32 v194, 1.0, v194\n"
"v_mfma_f32_32x32x16_bf16 v[144:159], v[212:215], v[88:91], v[144:159]\n"
"s_waitcnt lgkmcnt(4)\n"
"ds_read_b64_tr_b16 v[208:209], v220 offset:17920\n"
"ds_read_b64_tr_b16 v[210:211], v220 offset:18048\n"
"v_rcp_f32 v193, v193\n"
"v_add_f32 v195, 1.0, v195\n"
"v_mfma_f32_32x32x16_bf16 v[144:159], v[216:219], v[92:95], v[144:159]\n"
"ds_read_b64_tr_b16 v[212:213], v220 offset:21760\n"
"ds_read_b64_tr_b16 v[214:215], v220 offset:21888\n"
"v_mul_f32 v173, v173, v193\n"
"v_rcp_f32 v194, v194\n"
"v_mfma_f32_32x32x16_bf16 v[144:159], v[196:199], v[96:99], v[144:159]\n"
"s_waitcnt lgkmcnt(4)\n"
"ds_read_b64_tr_b16 v[216:217], v220 offset:22272\n"
"ds_read_b64_tr_b16 v[218:219], v220 offset:22400\n"
"v_mul_f32 v174, v174, v194\n"
"v_rcp_f32 v195, v195\n"
"v_mfma_f32_32x32x16_bf16 v[144:159], v[200:203], v[100:103], v[144:159]\n"
"s_add_u32 m0, s54, 21760\n"
"ds_read_b64_tr_b16 v[196:197], v220 offset:26112\n"
"ds_read_b64_tr_b16 v[198:199], v220 offset:26240\n"
"buffer_load_dwordx4 v222, s[40:43], s60 offen lds\n"
"v_cvt_pk_bf16_f32 v185, v162, v163\n"
"v_mul_f32 v175, v175, v195\n"
"v_mfma_f32_32x32x16_bf16 v[144:159], v[204:207], v[104:107], v[144:159]\n"
"s_waitcnt lgkmcnt(4)\n"
"ds_read_b64_tr_b16 v[200:201], v220 offset:26624\n"
"ds_read_b64_tr_b16 v[202:203], v220 offset:26752\n"
"v_cvt_pk_bf16_f32 v186, v164, v165\n"
"v_cvt_pk_bf16_f32 v187, v166, v167\n"
"v_cvt_pk_bf16_f32 v188, v168, v169\n"
"v_mfma_f32_32x32x16_bf16 v[144:159], v[208:211], v[108:111], v[144:159]\n"
"ds_read_b64_tr_b16 v[204:205], v220 offset:30464\n"
"ds_read_b64_tr_b16 v[206:207], v220 offset:30592\n"
"v_cvt_pk_bf16_f32 v189, v170, v171\n"
"v_cvt_pk_bf16_f32 v190, v172, v173\n"
"v_cvt_pk_bf16_f32 v191, v174, v175\n"
"v_mfma_f32_32x32x16_bf16 v[144:159], v[212:215], v[112:115], v[144:159]\n"
"s_waitcnt lgkmcnt(4)\n"
"ds_read_b64_tr_b16 v[208:209], v220 offset:30976\n"
"ds_read_b64_tr_b16 v[210:211], v220 offset:31104\n"
"v_mfma_f32_32x32x16_bf16 v[144:159], v[216:219], v[116:119], v[144:159]\n"
"s_add_u32 m0, s54, 26112\n"
"ds_read_b64_tr_b16 v[212:213], v220 offset:34816\n"
"ds_read_b64_tr_b16 v[214:215], v220 offset:34944\n"
"buffer_load_dwordx4 v222, s[40:43], s61 offen lds\n"
"v_mfma_f32_32x32x16_bf16 v[144:159], v[196:199], v[120:123], v[144:159]\n"
"s_waitcnt lgkmcnt(4)\n"
"ds_read_b64_tr_b16 v[216:217], v220 offset:35328\n"
"ds_read_b64_tr_b16 v[218:219], v220 offset:35456\n"
"v_add_u32 v220, s55, v221\n"
"v_mfma_f32_32x32x16_bf16 v[144:159], v[200:203], v[124:127], v[144:159]\n"
"ds_read_b64_tr_b16 v[196:197], v220 offset:64\n"
"ds_read_b64_tr_b16 v[198:199], v220 offset:192\n"
"v_mfma_f32_32x32x16_bf16 v[144:159], v[204:207], v[128:131], v[144:159]\n"
"s_waitcnt lgkmcnt(4)\n"
"ds_read_b64_tr_b16 v[200:201], v220 offset:576\n"
"ds_read_b64_tr_b16 v[202:203], v220 offset:704\n"
"v_mfma_f32_32x32x16_bf16 v[144:159], v[208:211], v[132:135], v[144:159]\n"
"s_add_u32 m0, s54, 30464\n"
"ds_read_b64_tr_b16 v[204:205], v220 offset:4416\n"
"ds_read_b64_tr_b16 v[206:207], v220 offset:4544\n"
"buffer_load_dwordx4 v222, s[40:43], s62 offen lds\n"
"v_mfma_f32_32x32x16_bf16 v[144:159], v[212:215], v[136:139], v[144:159]\n"
"s_waitcnt lgkmcnt(4)\n"
"ds_read_b64_tr_b16 v[208:209], v220 offset:4928\n"
"ds_read_b64_tr_b16 v[210:211], v220 offset:5056\n"
"v_lshlrev_b32 v192, 16, v176\n"
"v_and_b32 v193, s78, v176\n"
"v_lshlrev_b32 v194, 16, v177\n"
"v_mfma_f32_32x32x16_bf16 v[144:159], v[216:219], v[140:143], v[144:159]\n"
"ds_read_b64_tr_b16 v[212:213], v220 offset:8768\n"
"ds_read_b64_tr_b16 v[214:215], v220 offset:8896\n"
"v_and_b32 v195, s78, v177\n"
"v_mfma_f32_32x32x16_bf16 v[160:175], v[196:199], v[0:3], 0\n"
"s_waitcnt lgkmcnt(4)\n"
"ds_read_b64_tr_b16 v[216:217], v220 offset:9280\n"
"ds_read_b64_tr_b16 v[218:219], v220 offset:9408\n"
"v_mfma_f32_32x32x16_bf16 v[160:175], v[200:203], v[4:7], v[160:175]\n"
"s_add_u32 m0, s54, 34816\n"
"ds_read_b64_tr_b16 v[196:197], v220 offset:13120\n"
"ds_read_b64_tr_b16 v[198:199], v220 offset:13248\n"
"buffer_load_dwordx4 v222, s[40:43], s63 offen lds\n"
"v_mfma_f32_32x32x16_bf16 v[160:175], v[204:207], v[8:11], v[160:175]\n"
"s_waitcnt lgkmcnt(4)\n"
"ds_read_b64_tr_b16 v[200:201], v220 offset:13632\n"
"ds_read_b64_tr_b16 v[202:203], v220 offset:13760\n"
"v_mul_f32 v192, v192, v144\n"
"v_mul_f32 v193, v193, v145\n"
"v_mul_f32 v194, v194, v146\n"
"v_mfma_f32_32x32x16_bf16 v[160:175], v[208:211], v[12:15], v[160:175]\n"
"ds_read_b64_tr_b16 v[204:205], v220 offset:17472\n"
"ds_read_b64_tr_b16 v[206:207], v220 offset:17600\n"
"v_cvt_pk_bf16_f32 v176, v192, v193\n"
"v_mul_f32 v195, v195, v147\n"
"v_lshlrev_b32 v192, 16, v178\n"
"v_mfma_f32_32x32x16_bf16 v[160:175], v[212:215], v[16:19], v[160:175]\n"
"s_waitcnt lgkmcnt(4)\n"
"ds_read_b64_tr_b16 v[208:209], v220 offset:17984\n"
"ds_read_b64_tr_b16 v[210:211], v220 offset:18112\n"
"v_and_b32 v193, s78, v178\n"
"v_mul_f32 v192, v192, v148\n"
"v_mul_f32 v193, v193, v149\n"
"v_mfma_f32_32x32x16_bf16 v[160:175], v[216:219], v[20:23], v[160:175]\n"
"ds_read_b64_tr_b16 v[212:213], v220 offset:21824\n"
"ds_read_b64_tr_b16 v[214:215], v220 offset:21952\n"
"v_cvt_pk_bf16_f32 v177, v194, v195\n"
"v_lshlrev_b32 v194, 16, v179\n"
"v_and_b32 v195, s78, v179\n"
"v_mfma_f32_32x32x16_bf16 v[160:175], v[196:199], v[24:27], v[160:175]\n"
"s_waitcnt lgkmcnt(4)\n"
"ds_read_b64_tr_b16 v[216:217], v220 offset:22336\n"
"ds_read_b64_tr_b16 v[218:219], v220 offset:22464\n"
"v_cvt_pk_bf16_f32 v178, v192, v193\n"
"v_mul_f32 v194, v194, v150\n"
"v_mul_f32 v195, v195, v151\n"
"v_mfma_f32_32x32x16_bf16 v[160:175], v[200:203], v[28:31], v[160:175]\n"
"ds_read_b64_tr_b16 v[196:197], v220 offset:26176\n"
"ds_read_b64_tr_b16 v[198:199], v220 offset:26304\n"
"v_lshlrev_b32 v192, 16, v180\n"
"v_and_b32 v193, s78, v180\n"
"v_mul_f32 v192, v192, v152\n"
"v_mfma_f32_32x32x16_bf16 v[160:175], v[204:207], v[32:35], v[160:175]\n"
"s_waitcnt lgkmcnt(4)\n"
"ds_read_b64_tr_b16 v[200:201], v220 offset:26688\n"
"ds_read_b64_tr_b16 v[202:203], v220 offset:26816\n"
"v_cvt_pk_bf16_f32 v179, v194, v195\n"
"v_mul_f32 v193, v193, v153\n"
"v_lshlrev_b32 v194, 16, v181\n"
"v_mfma_f32_32x32x16_bf16 v[160:175], v[208:211], v[36:39], v[160:175]\n"
"ds_read_b64_tr_b16 v[204:205], v220 offset:30528\n"
"ds_read_b64_tr_b16 v[206:207], v220 offset:30656\n"
"v_and_b32 v195, s78, v181\n"
"v_mul_f32 v194, v194, v154\n"
"v_mul_f32 v195, v195, v155\n"
"v_mfma_f32_32x32x16_bf16 v[160:175], v[212:215], v[40:43], v[160:175]\n"
"s_waitcnt lgkmcnt(4)\n"
"ds_read_b64_tr_b16 v[208:209], v220 offset:31040\n"
"ds_read_b64_tr_b16 v[210:211], v220 offset:31168\n"
"v_cvt_pk_bf16_f32 v180, v192, v193\n"
"v_lshlrev_b32 v192, 16, v182\n"
"v_and_b32 v193, s78, v182\n"
"v_mfma_f32_32x32x16_bf16 v[160:175], v[216:219], v[44:47], v[160:175]\n"
"ds_read_b64_tr_b16 v[212:213], v220 offset:34880\n"
"ds_read_b64_tr_b16 v[214:215], v220 offset:35008\n"
"v_cvt_pk_bf16_f32 v181, v194, v195\n"
"v_mul_f32 v192, v192, v156\n"
"v_mul_f32 v193, v193, v157\n"
"v_mfma_f32_32x32x16_bf16 v[160:175], v[196:199], v[48:51], v[160:175]\n"
"s_waitcnt lgkmcnt(4)\n"
"ds_read_b64_tr_b16 v[216:217], v220 offset:35392\n"
"ds_read_b64_tr_b16 v[218:219], v220 offset:35520\n"
"v_add_u32 v220, s52, v221\n"
"v_lshlrev_b32 v194, 16, v183\n"
"v_and_b32 v195, s78, v183\n"
"v_mfma_f32_32x32x16_bf16 v[160:175], v[200:203], v[52:55], v[160:175]\n"
"ds_read_b64_tr_b16 v[196:197], v220 offset:64\n"
"ds_read_b64_tr_b16 v[198:199], v220 offset:192\n"
"v_cvt_pk_bf16_f32 v182, v192, v193\n"
"v_mul_f32 v194, v194, v158\n"
"v_mul_f32 v195, v195, v159\n"
"v_mfma_f32_32x32x16_bf16 v[160:175], v[204:207], v[56:59], v[160:175]\n"
"s_waitcnt lgkmcnt(4)\n"
"ds_read_b64_tr_b16 v[200:201], v220 offset:576\n"
"ds_read_b64_tr_b16 v[202:203], v220 offset:704\n"
"v_cvt_pk_bf16_f32 v183, v194, v195\n"
"v_mfma_f32_32x32x16_bf16 v[160:175], v[208:211], v[60:63], v[160:175]\n"
"ds_read_b64_tr_b16 v[204:205], v220 offset:4416\n"
"ds_read_b64_tr_b16 v[206:207], v220 offset:4544\n"
"v_mfma_f32_32x32x16_bf16 v[160:175], v[212:215], v[64:67], v[160:175]\n"
"s_waitcnt lgkmcnt(4)\n"
"ds_read_b64_tr_b16 v[208:209], v220 offset:4928\n"
"ds_read_b64_tr_b16 v[210:211], v220 offset:5056\n"
"v_mfma_f32_32x32x16_bf16 v[160:175], v[216:219], v[68:71], v[160:175]\n"
"ds_read_b64_tr_b16 v[212:213], v220 offset:8768\n"
"ds_read_b64_tr_b16 v[214:215], v220 offset:8896\n"
"v_mfma_f32_32x32x16_bf16 v[160:175], v[196:199], v[72:75], v[160:175]\n"
"s_waitcnt lgkmcnt(4)\n"
"ds_read_b64_tr_b16 v[216:217], v220 offset:9280\n"
"ds_read_b64_tr_b16 v[218:219], v220 offset:9408\n"
"v_mfma_f32_32x32x16_bf16 v[160:175], v[200:203], v[76:79], v[160:175]\n"
"ds_read_b64_tr_b16 v[196:197], v220 offset:13120\n"
"ds_read_b64_tr_b16 v[198:199], v220 offset:13248\n"
"v_mfma_f32_32x32x16_bf16 v[160:175], v[204:207], v[80:83], v[160:175]\n"
"s_waitcnt lgkmcnt(4)\n"
"ds_read_b64_tr_b16 v[200:201], v220 offset:13632\n"
"ds_read_b64_tr_b16 v[202:203], v220 offset:13760\n"
"v_mfma_f32_32x32x16_bf16 v[160:175], v[208:211], v[84:87], v[160:175]\n"
"ds_read_b64_tr_b16 v[204:205], v220 offset:17472\n"
"ds_read_b64_tr_b16 v[206:207], v220 offset:17600\n"
"v_mfma_f32_32x32x16_bf16 v[160:175], v[212:215], v[88:91], v[160:175]\n"
"s_waitcnt lgkmcnt(4)\n"
"ds_read_b64_tr_b16 v[208:209], v220 offset:17984\n"
"ds_read_b64_tr_b16 v[210:211], v220 offset:18112\n"
"v_mfma_f32_32x32x16_bf16 v[160:175], v[216:219], v[92:95], v[160:175]\n"
"ds_read_b64_tr_b16 v[212:213], v220 offset:21824\n"
"ds_read_b64_tr_b16 v[214:215], v220 offset:21952\n"
"v_mfma_f32_32x32x16_bf16 v[160:175], v[196:199], v[96:99], v[160:175]\n"
"s_waitcnt lgkmcnt(4)\n"
"ds_read_b64_tr_b16 v[216:217], v220 offset:22336\n"
"ds_read_b64_tr_b16 v[218:219], v220 offset:22464\n"
"v_mfma_f32_32x32x16_bf16 v[160:175], v[200:203], v[100:103], v[160:175]\n"
"ds_read_b64_tr_b16 v[196:197], v220 offset:26176\n"
"ds_read_b64_tr_b16 v[198:199], v220 offset:26304\n"
"v_mfma_f32_32x32x16_bf16 v[160:175], v[204:207], v[104:107], v[160:175]\n"
"s_waitcnt lgkmcnt(4)\n"
"ds_read_b64_tr_b16 v[200:201], v220 offset:26688\n"
"ds_read_b64_tr_b16 v[202:203], v220 offset:26816\n"
"v_mfma_f32_32x32x16_bf16 v[160:175], v[208:211], v[108:111], v[160:175]\n"
"ds_read_b64_tr_b16 v[204:205], v220 offset:30528\n"
"ds_read_b64_tr_b16 v[206:207], v220 offset:30656\n"
"v_mfma_f32_32x32x16_bf16 v[160:175], v[212:215], v[112:115], v[160:175]\n"
"s_waitcnt lgkmcnt(4)\n"
"ds_read_b64_tr_b16 v[208:209], v220 offset:31040\n"
"ds_read_b64_tr_b16 v[210:211], v220 offset:31168\n"
"v_mfma_f32_32x32x16_bf16 v[160:175], v[216:219], v[116:119], v[160:175]\n"
"ds_read_b64_tr_b16 v[212:213], v220 offset:34880\n"
"ds_read_b64_tr_b16 v[214:215], v220 offset:35008\n"
"v_mfma_f32_32x32x16_bf16 v[160:175], v[196:199], v[120:123], v[160:175]\n"
"s_waitcnt lgkmcnt(4)\n"
"ds_read_b64_tr_b16 v[216:217], v220 offset:35392\n"
"ds_read_b64_tr_b16 v[218:219], v220 offset:35520\n"
"v_add_u32 v220, s53, v221\n"
"v_mfma_f32_32x32x16_bf16 v[160:175], v[200:203], v[124:127], v[160:175]\n"
"s_waitcnt vmcnt(9)\n"
"s_barrier\n"
"ds_read_b64_tr_b16 v[196:197], v220 offset:0\n"
"ds_read_b64_tr_b16 v[198:199], v220 offset:128\n"
"v_mfma_f32_32x32x16_bf16 v[160:175], v[204:207], v[128:131], v[160:175]\n"
"s_waitcnt lgkmcnt(4)\n"
"ds_read_b64_tr_b16 v[200:201], v220 offset:64\n"
"ds_read_b64_tr_b16 v[202:203], v220 offset:192\n"
"v_mfma_f32_32x32x16_bf16 v[160:175], v[208:211], v[132:135], v[160:175]\n"
"s_add_u32 m0, s55, 0\n"
"ds_read_b64_tr_b16 v[204:205], v220 offset:4352\n"
"ds_read_b64_tr_b16 v[206:207], v220 offset:4480\n"
"buffer_load_dwordx4 v222, s[40:43], s64 offen lds\n"
"v_mfma_f32_32x32x16_bf16 v[160:175], v[212:215], v[136:139], v[160:175]\n"
"s_waitcnt lgkmcnt(4)\n"
"ds_read_b64_tr_b16 v[208:209], v220 offset:4416\n"
"ds_read_b64_tr_b16 v[210:211], v220 offset:4544\n"
"v_lshlrev_b32 v192, 16, v184\n"
"v_and_b32 v193, s78, v184\n"
"v_lshlrev_b32 v194, 16, v185\n"
"v_mfma_f32_32x32x16_bf16 v[160:175], v[216:219], v[140:143], v[160:175]\n"
"s_add_u32 m0, s55, 4352\n"
"ds_read_b64_tr_b16 v[212:213], v220 offset:8704\n"
"ds_read_b64_tr_b16 v[214:215], v220 offset:8832\n"
"buffer_load_dwordx4 v222, s[40:43], s65 offen lds\n"
"v_and_b32 v195, s78, v185\n"
"v_mfma_f32_32x32x16_bf16 a[0:15], v[196:199], v[176:179], a[0:15]\n"
"s_waitcnt lgkmcnt(4)\n"
"ds_read_b64_tr_b16 v[216:217], v220 offset:8768\n"
"ds_read_b64_tr_b16 v[218:219], v220 offset:8896\n"
"v_mfma_f32_32x32x16_bf16 a[16:31], v[200:203], v[176:179], a[16:31]\n"
"s_add_u32 m0, s55, 8704\n"
"ds_read_b64_tr_b16 v[196:197], v220 offset:13056\n"
"ds_read_b64_tr_b16 v[198:199], v220 offset:13184\n"
"buffer_load_dwordx4 v222, s[40:43], s66 offen lds\n"
"v_mfma_f32_32x32x16_bf16 a[32:47], v[204:207], v[176:179], a[32:47]\n"
"s_waitcnt lgkmcnt(4)\n"
"ds_read_b64_tr_b16 v[200:201], v220 offset:13120\n"
"ds_read_b64_tr_b16 v[202:203], v220 offset:13248\n"
"v_mul_f32 v192, v192, v160\n"
"v_mul_f32 v193, v193, v161\n"
"v_mul_f32 v194, v194, v162\n"
"v_mfma_f32_32x32x16_bf16 a[48:63], v[208:211], v[176:179], a[48:63]\n"
"s_add_u32 m0, s55, 13056\n"
"ds_read_b64_tr_b16 v[204:205], v220 offset:17408\n"
"ds_read_b64_tr_b16 v[206:207], v220 offset:17536\n"
"buffer_load_dwordx4 v222, s[40:43], s67 offen lds\n"
"v_cvt_pk_bf16_f32 v184, v192, v193\n"
"v_mul_f32 v195, v195, v163\n"
"v_mfma_f32_32x32x16_bf16 a[64:79], v[212:215], v[176:179], a[64:79]\n"
"s_waitcnt lgkmcnt(4)\n"
"ds_read_b64_tr_b16 v[208:209], v220 offset:17472\n"
"ds_read_b64_tr_b16 v[210:211], v220 offset:17600\n"
"v_lshlrev_b32 v192, 16, v186\n"
"v_and_b32 v193, s78, v186\n"
"v_mul_f32 v192, v192, v164\n"
"v_mfma_f32_32x32x16_bf16 a[80:95], v[216:219], v[176:179], a[80:95]\n"
"s_add_u32 m0, s55, 17408\n"
"ds_read_b64_tr_b16 v[212:213], v220 offset:21760\n"
"ds_read_b64_tr_b16 v[214:215], v220 offset:21888\n"
"buffer_load_dwordx4 v222, s[40:43], s68 offen lds\n"
"v_cvt_pk_bf16_f32 v185, v194, v195\n"
"v_mul_f32 v193, v193, v165\n"
"v_mfma_f32_32x32x16_bf16 a[96:111], v[196:199], v[176:179], a[96:111]\n"
"s_waitcnt lgkmcnt(4)\n"
"ds_read_b64_tr_b16 v[216:217], v220 offset:21824\n"
"ds_read_b64_tr_b16 v[218:219], v220 offset:21952\n"
"v_lshlrev_b32 v194, 16, v187\n"
"v_and_b32 v195, s78, v187\n"
"v_mul_f32 v194, v194, v166\n"
"v_mfma_f32_32x32x16_bf16 a[112:127], v[200:203], v[176:179], a[112:127]\n"
"s_add_u32 m0, s55, 21760\n"
"ds_read_b64_tr_b16 v[196:197], v220 offset:26112\n"
"ds_read_b64_tr_b16 v[198:199], v220 offset:26240\n"
"buffer_load_dwordx4 v222, s[40:43], s69 offen lds\n"
"v_cvt_pk_bf16_f32 v186, v192, v193\n"
"v_mul_f32 v195, v195, v167\n"
"v_mfma_f32_32x32x16_bf16 a[128:143], v[204:207], v[176:179], a[128:143]\n"
"s_waitcnt lgkmcnt(4)\n"
"ds_read_b64_tr_b16 v[200:201], v220 offset:26176\n"
"ds_read_b64_tr_b16 v[202:203], v220 offset:26304\n"
"v_lshlrev_b32 v192, 16, v188\n"
"v_and_b32 v193, s78, v188\n"
"v_mul_f32 v192, v192, v168\n"
"v_mfma_f32_32x32x16_bf16 a[144:159], v[208:211], v[176:179], a[144:159]\n"
"s_add_u32 m0, s55, 26112\n"
"ds_read_b64_tr_b16 v[204:205], v220 offset:30464\n"
"ds_read_b64_tr_b16 v[206:207], v220 offset:30592\n"
"buffer_load_dwordx4 v222, s[40:43], s70 offen lds\n"
"v_cvt_pk_bf16_f32 v187, v194, v195\n"
"v_mul_f32 v193, v193, v169\n"
"v_mfma_f32_32x32x16_bf16 a[160:175], v[212:215], v[176:179], a[160:175]\n"
"s_waitcnt lgkmcnt(4)\n"
"ds_read_b64_tr_b16 v[208:209], v220 offset:30528\n"
"ds_read_b64_tr_b16 v[210:211], v220 offset:30656\n"
"v_lshlrev_b32 v194, 16, v189\n"
"v_and_b32 v195, s78, v189\n"
"v_mul_f32 v194, v194, v170\n"
"v_mfma_f32_32x32x16_bf16 a[176:191], v[216:219], v[176:179], a[176:191]\n"
"s_add_u32 m0, s55, 30464\n"
"ds_read_b64_tr_b16 v[212:213], v220 offset:34816\n"
"ds_read_b64_tr_b16 v[214:215], v220 offset:34944\n"
"buffer_load_dwordx4 v222, s[40:43], s71 offen lds\n"
"v_cvt_pk_bf16_f32 v188, v192, v193\n"
"v_mul_f32 v195, v195, v171\n"
"v_mfma_f32_32x32x16_bf16 a[192:207], v[196:199], v[176:179], a[192:207]\n"
"s_waitcnt lgkmcnt(4)\n"
"ds_read_b64_tr_b16 v[216:217], v220 offset:34880\n"
"ds_read_b64_tr_b16 v[218:219], v220 offset:35008\n"
"v_lshlrev_b32 v192, 16, v190\n"
"v_and_b32 v193, s78, v190\n"
"v_mul_f32 v192, v192, v172\n"
"v_mfma_f32_32x32x16_bf16 a[208:223], v[200:203], v[176:179], a[208:223]\n"
"s_add_u32 m0, s55, 34816\n"
"ds_read_b64_tr_b16 v[196:197], v220 offset:512\n"
"ds_read_b64_tr_b16 v[198:199], v220 offset:640\n"
"buffer_load_dwordx4 v222, s[40:43], s72 offen lds\n"
"v_cvt_pk_bf16_f32 v189, v194, v195\n"
"v_mul_f32 v193, v193, v173\n"
"v_mfma_f32_32x32x16_bf16 a[224:239], v[204:207], v[176:179], a[224:239]\n"
"s_waitcnt lgkmcnt(4)\n"
"ds_read_b64_tr_b16 v[200:201], v220 offset:576\n"
"ds_read_b64_tr_b16 v[202:203], v220 offset:704\n"
"v_lshlrev_b32 v194, 16, v191\n"
"v_and_b32 v195, s78, v191\n"
"v_mul_f32 v194, v194, v174\n"
"v_mfma_f32_32x32x16_bf16 a[240:255], v[208:211], v[176:179], a[240:255]\n"
"s_add_u32 m0, s52, 0\n"
"ds_read_b64_tr_b16 v[204:205], v220 offset:4864\n"
"ds_read_b64_tr_b16 v[206:207], v220 offset:4992\n"
"buffer_load_dwordx4 v223, s[48:51], 0 offen lds\n"
"v_cvt_pk_bf16_f32 v190, v192, v193\n"
"v_mul_f32 v195, v195, v175\n"
"v_mfma_f32_32x32x16_bf16 v[224:239], v[212:215], v[176:179], v[224:239]\n"
"s_waitcnt lgkmcnt(4)\n"
"ds_read_b64_tr_b16 v[208:209], v220 offset:4928\n"
"ds_read_b64_tr_b16 v[210:211], v220 offset:5056\n"
"v_cvt_pk_bf16_f32 v191, v194, v195\n"
"v_mfma_f32_32x32x16_bf16 v[240:255], v[216:219], v[176:179], v[240:255]\n"
"s_add_u32 m0, s52, 4224\n"
"ds_read_b64_tr_b16 v[212:213], v220 offset:9216\n"
"ds_read_b64_tr_b16 v[214:215], v220 offset:9344\n"
"buffer_load_dwordx4 v223, s[48:51], 0 offen offset:128 lds\n"
"v_mfma_f32_32x32x16_bf16 a[0:15], v[196:199], v[180:183], a[0:15]\n"
"s_waitcnt lgkmcnt(4)\n"
"ds_read_b64_tr_b16 v[216:217], v220 offset:9280\n"
"ds_read_b64_tr_b16 v[218:219], v220 offset:9408\n"
"v_mfma_f32_32x32x16_bf16 a[16:31], v[200:203], v[180:183], a[16:31]\n"
"s_add_u32 m0, s52, 8448\n"
"ds_read_b64_tr_b16 v[196:197], v220 offset:13568\n"
"ds_read_b64_tr_b16 v[198:199], v220 offset:13696\n"
"buffer_load_dwordx4 v223, s[48:51], 0 offen offset:256 lds\n"
"v_mfma_f32_32x32x16_bf16 a[32:47], v[204:207], v[180:183], a[32:47]\n"
"s_waitcnt lgkmcnt(4)\n"
"ds_read_b64_tr_b16 v[200:201], v220 offset:13632\n"
"ds_read_b64_tr_b16 v[202:203], v220 offset:13760\n"
"v_mfma_f32_32x32x16_bf16 a[48:63], v[208:211], v[180:183], a[48:63]\n"
"s_add_u32 m0, s52, 12672\n"
"ds_read_b64_tr_b16 v[204:205], v220 offset:17920\n"
"ds_read_b64_tr_b16 v[206:207], v220 offset:18048\n"
"buffer_load_dwordx4 v223, s[48:51], 0 offen offset:384 lds\n"
"v_mfma_f32_32x32x16_bf16 a[64:79], v[212:215], v[180:183], a[64:79]\n"
"s_waitcnt lgkmcnt(4)\n"
"ds_read_b64_tr_b16 v[208:209], v220 offset:17984\n"
"ds_read_b64_tr_b16 v[210:211], v220 offset:18112\n"
"v_mfma_f32_32x32x16_bf16 a[80:95], v[216:219], v[180:183], a[80:95]\n"
"s_add_u32 m0, s52, 16896\n"
"ds_read_b64_tr_b16 v[212:213], v220 offset:22272\n"
"ds_read_b64_tr_b16 v[214:215], v220 offset:22400\n"
"buffer_load_dwordx4 v223, s[48:51], 0 offen offset:512 lds\n"
"v_mfma_f32_32x32x16_bf16 a[96:111], v[196:199], v[180:183], a[96:111]\n"
"s_waitcnt lgkmcnt(4)\n"
"ds_read_b64_tr_b16 v[216:217], v220 offset:22336\n"
"ds_read_b64_tr_b16 v[218:219], v220 offset:22464\n"
"v_mfma_f32_32x32x16_bf16 a[112:127], v[200:203], v[180:183], a[112:127]\n"
"s_add_u32 m0, s52, 21120\n"
"ds_read_b64_tr_b16 v[196:197], v220 offset:26624\n"
"ds_read_b64_tr_b16 v[198:199], v220 offset:26752\n"
"buffer_load_dwordx4 v223, s[48:51], 0 offen offset:640 lds\n"
"v_mfma_f32_32x32x16_bf16 a[128:143], v[204:207], v[180:183], a[128:143]\n"
"s_waitcnt lgkmcnt(4)\n"
"ds_read_b64_tr_b16 v[200:201], v220 offset:26688\n"
"ds_read_b64_tr_b16 v[202:203], v220 offset:26816\n"
"v_mfma_f32_32x32x16_bf16 a[144:159], v[208:211], v[180:183], a[144:159]\n"
"s_add_u32 m0, s52, 25344\n"
"ds_read_b64_tr_b16 v[204:205], v220 offset:30976\n"
"ds_read_b64_tr_b16 v[206:207], v220 offset:31104\n"
"buffer_load_dwordx4 v223, s[48:51], 0 offen offset:768 lds\n"
"v_mfma_f32_32x32x16_bf16 a[160:175], v[212:215], v[180:183], a[160:175]\n"
"s_waitcnt lgkmcnt(4)\n"
"ds_read_b64_tr_b16 v[208:209], v220 offset:31040\n"
"ds_read_b64_tr_b16 v[210:211], v220 offset:31168\n"
"v_mfma_f32_32x32x16_bf16 a[176:191], v[216:219], v[180:183], a[176:191]\n"
"s_sub_u32 s74, s74, 1\n"
"ds_read_b64_tr_b16 v[212:213], v220 offset:35328\n"
"ds_read_b64_tr_b16 v[214:215], v220 offset:35456\n"
"v_mfma_f32_32x32x16_bf16 a[192:207], v[196:199], v[180:183], a[192:207]\n"
"s_waitcnt lgkmcnt(4)\n"
"ds_read_b64_tr_b16 v[216:217], v220 offset:35392\n"
"ds_read_b64_tr_b16 v[218:219], v220 offset:35520\n"
"v_add_u32 v220, s54, v221\n"
"v_mfma_f32_32x32x16_bf16 a[208:223], v[200:203], v[180:183], a[208:223]\n"
"s_waitcnt vmcnt(16)\n"
"s_barrier\n"
"ds_read_b64_tr_b16 v[196:197], v220 offset:0\n"
"ds_read_b64_tr_b16 v[198:199], v220 offset:128\n"
"v_mfma_f32_32x32x16_bf16 a[224:239], v[204:207], v[180:183], a[224:239]\n"
"s_waitcnt lgkmcnt(4)\n"
"ds_read_b64_tr_b16 v[200:201], v220 offset:512\n"
"ds_read_b64_tr_b16 v[202:203], v220 offset:640\n"
"v_mfma_f32_32x32x16_bf16 a[240:255], v[208:211], v[180:183], a[240:255]\n"
"s_add_u32 m0, s52, 29568\n"
"ds_read_b64_tr_b16 v[204:205], v220 offset:4352\n"
"ds_read_b64_tr_b16 v[206:207], v220 offset:4480\n"
"buffer_load_dwordx4 v223, s[48:51], 0 offen offset:896 lds\n"
"v_mfma_f32_32x32x16_bf16 v[224:239], v[212:215], v[180:183], v[224:239]\n"
"s_waitcnt lgkmcnt(4)\n"
"ds_read_b64_tr_b16 v[208:209], v220 offset:4864\n"
"ds_read_b64_tr_b16 v[210:211], v220 offset:4992\n"
"v_mfma_f32_32x32x16_bf16 v[240:255], v[216:219], v[180:183], v[240:255]\n"
"s_add_u32 m0, s52, 33792\n"
"ds_read_b64_tr_b16 v[212:213], v220 offset:8704\n"
"ds_read_b64_tr_b16 v[214:215], v220 offset:8832\n"
"buffer_load_dwordx4 v223, s[48:51], 0 offen offset:1024 lds\n"
"v_add_u32 v223, s77, v223\n"
"s_mov_b64 s[80:81], s[52:53]\n"
"s_mov_b64 s[52:53], s[54:55]\n"
"s_mov_b64 s[54:55], s[80:81]\n"
"s_cmp_lg_u32 s74, 0\n"
"s_waitcnt lgkmcnt(8)\n"
"s_cbranch_scc1 1b\n"
"s_waitcnt lgkmcnt(0)\n"
"s_mov_b64 s[82:83], %[pp]\n"
"v_mbcnt_lo_u32_b32 v192, -1, 0\n"
"v_mbcnt_hi_u32_b32 v192, -1, v192\n"
"v_lshlrev_b32 v193, 4, v192\n"
"s_waitcnt vmcnt(0)\n"
"s_barrier\n"
"v_add_u32 v220, s54, v221\n"
"ds_read_b64_tr_b16 v[196:197], v220 offset:0\n"
"ds_read_b64_tr_b16 v[198:199], v220 offset:128\n"
"ds_read_b64_tr_b16 v[200:201], v220 offset:512\n"
"ds_read_b64_tr_b16 v[202:203], v220 offset:640\n"
"ds_read_b64_tr_b16 v[204:205], v220 offset:64\n"
"ds_read_b64_tr_b16 v[206:207], v220 offset:192\n"
"ds_read_b64_tr_b16 v[208:209], v220 offset:576\n"
"ds_read_b64_tr_b16 v[210:211], v220 offset:704\n"
"ds_read_b64_tr_b16 v[212:213], v220 offset:4352\n"
"ds_read_b64_tr_b16 v[214:215], v220 offset:4480\n"
"s_waitcnt lgkmcnt(8)\n"
"v_mfma_f32_32x32x16_bf16 a[0:15], v[196:199], v[184:187], a[0:15]\n"
"ds_read_b64_tr_b16 v[216:217], v220 offset:4864\n"
"ds_read_b64_tr_b16 v[218:219], v220 offset:4992\n"
"s_waitcnt lgkmcnt(8)\n"
"v_mfma_f32_32x32x16_bf16 a[0:15], v[200:203], v[188:191], a[0:15]\n"
"ds_read_b64_tr_b16 v[196:197], v220 offset:4416\n"
"ds_read_b64_tr_b16 v[198:199], v220 offset:4544\n"
"s_waitcnt lgkmcnt(8)\n"
"v_mfma_f32_32x32x16_bf16 a[16:31], v[204:207], v[184:187], a[16:31]\n"
"ds_read_b64_tr_b16 v[200:201], v220 offset:4928\n"
"ds_read_b64_tr_b16 v[202:203], v220 offset:5056\n"
"s_waitcnt lgkmcnt(8)\n"
"v_mfma_f32_32x32x16_bf16 a[16:31], v[208:211], v[188:191], a[16:31]\n"
"ds_read_b64_tr_b16 v[204:205], v220 offset:8704\n"
"ds_read_b64_tr_b16 v[206:207], v220 offset:8832\n"
"s_waitcnt lgkmcnt(8)\n"
"v_mfma_f32_32x32x16_bf16 a[32:47], v[212:215], v[184:187], a[32:47]\n"
"ds_read_b64_tr_b16 v[208:209], v220 offset:9216\n"
"ds_read_b64_tr_b16 v[210:211], v220 offset:9344\n"
"s_waitcnt lgkmcnt(8)\n"
"v_mfma_f32_32x32x16_bf16 a[32:47], v[216:219], v[188:191], a[32:47]\n"
"ds_read_b64_tr_b16 v[212:213], v220 offset:8768\n"
"ds_read_b64_tr_b16 v[214:215], v220 offset:8896\n"
"v_accvgpr_read_b32 v144, a0\n"
"v_accvgpr_read_b32 v145, a1\n"
"v_accvgpr_read_b32 v146, a2\n"
"v_accvgpr_read_b32 v147, a3\n"
"v_accvgpr_read_b32 v148, a4\n"
"v_accvgpr_read_b32 v149, a5\n"
"v_accvgpr_read_b32 v150, a6\n"
"v_accvgpr_read_b32 v151, a7\n"
"v_accvgpr_read_b32 v152, a8\n"
"v_accvgpr_read_b32 v153, a9\n"
"v_accvgpr_read_b32 v154, a10\n"
"v_accvgpr_read_b32 v155, a11\n"
"v_accvgpr_read_b32 v156, a12\n"
"v_accvgpr_read_b32 v157, a13\n"
"v_accvgpr_read_b32 v158, a14\n"
"v_accvgpr_read_b32 v159, a15\n"
"v_cvt_pk_bf16_f32 v0, v144, v145\n"
"v_cvt_pk_bf16_f32 v1, v146, v147\n"
"v_cvt_pk_bf16_f32 v2, v148, v149\n"
"v_cvt_pk_bf16_f32 v3, v150, v151\n"
"v_cvt_pk_bf16_f32 v4, v152, v153\n"
"v_cvt_pk_bf16_f32 v5, v154, v155\n"
"v_cvt_pk_bf16_f32 v6, v156, v157\n"
"v_cvt_pk_bf16_f32 v7, v158, v159\n"
"global_store_dwordx4 v193, v[0:3], s[82:83] offset:0\n"
"global_store_dwordx4 v193, v[4:7], s[82:83] offset:1024\n"
"s_waitcnt lgkmcnt(8)\n"
"v_mfma_f32_32x32x16_bf16 a[48:63], v[196:199], v[184:187], a[48:63]\n"
"ds_read_b64_tr_b16 v[216:217], v220 offset:9280\n"
"ds_read_b64_tr_b16 v[218:219], v220 offset:9408\n"
"s_waitcnt lgkmcnt(8)\n"
"v_mfma_f32_32x32x16_bf16 a[48:63], v[200:203], v[188:191], a[48:63]\n"
"ds_read_b64_tr_b16 v[196:197], v220 offset:13056\n"
"ds_read_b64_tr_b16 v[198:199], v220 offset:13184\n"
"v_accvgpr_read_b32 v144, a16\n"
"v_accvgpr_read_b32 v145, a17\n"
"v_accvgpr_read_b32 v146, a18\n"
"v_accvgpr_read_b32 v147, a19\n"
"v_accvgpr_read_b32 v148, a20\n"
"v_accvgpr_read_b32 v149, a21\n"
"v_accvgpr_read_b32 v150, a22\n"
"v_accvgpr_read_b32 v151, a23\n"
"v_accvgpr_read_b32 v152, a24\n"
"v_accvgpr_read_b32 v153, a25\n"
"v_accvgpr_read_b32 v154, a26\n"
"v_accvgpr_read_b32 v155, a27\n"
"v_accvgpr_read_b32 v156, a28\n"
"v_accvgpr_read_b32 v157, a29\n"
"v_accvgpr_read_b32 v158, a30\n"
"v_accvgpr_read_b32 v159, a31\n"
"v_cvt_pk_bf16_f32 v8, v144, v145\n"
"v_cvt_pk_bf16_f32 v9, v146, v147\n"
"v_cvt_pk_bf16_f32 v10, v148, v149\n"
"v_cvt_pk_bf16_f32 v11, v150, v151\n"
"v_cvt_pk_bf16_f32 v12, v152, v153\n"
"v_cvt_pk_bf16_f32 v13, v154, v155\n"
"v_cvt_pk_bf16_f32 v14, v156, v157\n"
"v_cvt_pk_bf16_f32 v15, v158, v159\n"
"global_store_dwordx4 v193, v[8:11], s[82:83] offset:2048\n"
"global_store_dwordx4 v193, v[12:15], s[82:83] offset:3072\n"
"s_add_u32 s82, s82, 4096\n"
"s_addc_u32 s83, s83, 0\n"
"s_waitcnt lgkmcnt(8)\n"
"v_mfma_f32_32x32x16_bf16 a[64:79], v[204:207], v[184:187], a[64:79]\n"
"ds_read_b64_tr_b16 v[200:201], v220 offset:13568\n"
"ds_read_b64_tr_b16 v[202:203], v220 offset:13696\n"
"s_waitcnt lgkmcnt(8)\n"
"v_mfma_f32_32x32x16_bf16 a[64:79], v[208:211], v[188:191], a[64:79]\n"
"ds_read_b64_tr_b16 v[204:205], v220 offset:13120\n"
"ds_read_b64_tr_b16 v[206:207], v220 offset:13248\n"
"v_accvgpr_read_b32 v144, a32\n"
"v_accvgpr_read_b32 v145, a33\n"
"v_accvgpr_read_b32 v146, a34\n"
"v_accvgpr_read_b32 v147, a35\n"
"v_accvgpr_read_b32 v148, a36\n"
"v_accvgpr_read_b32 v149, a37\n"
"v_accvgpr_read_b32 v150, a38\n"
"v_accvgpr_read_b32 v151, a39\n"
"v_accvgpr_read_b32 v152, a40\n"
"v_accvgpr_read_b32 v153, a41\n"
"v_accvgpr_read_b32 v154, a42\n"
"v_accvgpr_read_b32 v155, a43\n"
"v_accvgpr_read_b32 v156, a44\n"
"v_accvgpr_read_b32 v157, a45\n"
"v_accvgpr_read_b32 v158, a46\n"
"v_accvgpr_read_b32 v159, a47\n"
"v_cvt_pk_bf16_f32 v16, v144, v145\n"
"v_cvt_pk_bf16_f32 v17, v146, v147\n"
"v_cvt_pk_bf16_f32 v18, v148, v149\n"
"v_cvt_pk_bf16_f32 v19, v150, v151\n"
"v_cvt_pk_bf16_f32 v20, v152, v153\n"
"v_cvt_pk_bf16_f32 v21, v154, v155\n"
"v_cvt_pk_bf16_f32 v22, v156, v157\n"
"v_cvt_pk_bf16_f32 v23, v158, v159\n"
"global_store_dwordx4 v193, v[16:19], s[82:83] offset:0\n"
"global_store_dwordx4 v193, v[20:23], s[82:83] offset:1024\n"
"s_waitcnt lgkmcnt(8)\n"
"v_mfma_f32_32x32x16_bf16 a[80:95], v[212:215], v[184:187], a[80:95]\n"
"ds_read_b64_tr_b16 v[208:209], v220 offset:13632\n"
"ds_read_b64_tr_b16 v[210:211], v220 offset:13760\n"
"s_waitcnt lgkmcnt(8)\n"
"v_mfma_f32_32x32x16_bf16 a[80:95], v[216:219], v[188:191], a[80:95]\n"
"ds_read_b64_tr_b16 v[212:213], v220 offset:17408\n"
"ds_read_b64_tr_b16 v[214:215], v220 offset:17536\n"
"v_accvgpr_read_b32 v144, a48\n"
"v_accvgpr_read_b32 v145, a49\n"
"v_accvgpr_read_b32 v146, a50\n"
"v_accvgpr_read_b32 v147, a51\n"
"v_accvgpr_read_b32 v148, a52\n"
"v_accvgpr_read_b32 v149, a53\n"
"v_accvgpr_read_b32 v150, a54\n"
"v_accvgpr_read_b32 v151, a55\n"
"v_accvgpr_read_b32 v152, a56\n"
"v_accvgpr_read_b32 v153, a57\n"
"v_accvgpr_read_b32 v154, a58\n"
"v_accvgpr_read_b32 v155, a59\n"
"v_accvgpr_read_b32 v156, a60\n"
"v_accvgpr_read_b32 v157, a61\n"
"v_accvgpr_read_b32 v158, a62\n"
"v_accvgpr_read_b32 v159, a63\n"
"v_cvt_pk_bf16_f32 v24, v144, v145\n"
"v_cvt_pk_bf16_f32 v25, v146, v147\n"
"v_cvt_pk_bf16_f32 v26, v148, v149\n"
"v_cvt_pk_bf16_f32 v27, v150, v151\n"
"v_cvt_pk_bf16_f32 v28, v152, v153\n"
"v_cvt_pk_bf16_f32 v29, v154, v155\n"
"v_cvt_pk_bf16_f32 v30, v156, v157\n"
"v_cvt_pk_bf16_f32 v31, v158, v159\n"
"global_store_dwordx4 v193, v[24:27], s[82:83] offset:2048\n"
"global_store_dwordx4 v193, v[28:31], s[82:83] offset:3072\n"
"s_add_u32 s82, s82, 4096\n"
"s_addc_u32 s83, s83, 0\n"
"s_waitcnt lgkmcnt(8)\n"
"v_mfma_f32_32x32x16_bf16 a[96:111], v[196:199], v[184:187], a[96:111]\n"
"ds_read_b64_tr_b16 v[216:217], v220 offset:17920\n"
"ds_read_b64_tr_b16 v[218:219], v220 offset:18048\n"
"s_waitcnt lgkmcnt(8)\n"
"v_mfma_f32_32x32x16_bf16 a[96:111], v[200:203], v[188:191], a[96:111]\n"
"ds_read_b64_tr_b16 v[196:197], v220 offset:17472\n"
"ds_read_b64_tr_b16 v[198:199], v220 offset:17600\n"
"v_accvgpr_read_b32 v144, a64\n"
"v_accvgpr_read_b32 v145, a65\n"
"v_accvgpr_read_b32 v146, a66\n"
"v_accvgpr_read_b32 v147, a67\n"
"v_accvgpr_read_b32 v148, a68\n"
"v_accvgpr_read_b32 v149, a69\n"
"v_accvgpr_read_b32 v150, a70\n"
"v_accvgpr_read_b32 v151, a71\n"
"v_accvgpr_read_b32 v152, a72\n"
"v_accvgpr_read_b32 v153, a73\n"
"v_accvgpr_read_b32 v154, a74\n"
"v_accvgpr_read_b32 v155, a75\n"
"v_accvgpr_read_b32 v156, a76\n"
"v_accvgpr_read_b32 v157, a77\n"
"v_accvgpr_read_b32 v158, a78\n"
"v_accvgpr_read_b32 v159, a79\n"
"v_cvt_pk_bf16_f32 v32, v144, v145\n"
"v_cvt_pk_bf16_f32 v33, v146, v147\n"
"v_cvt_pk_bf16_f32 v34, v148, v149\n"
"v_cvt_pk_bf16_f32 v35, v150, v151\n"
"v_cvt_pk_bf16_f32 v36, v152, v153\n"
"v_cvt_pk_bf16_f32 v37, v154, v155\n"
"v_cvt_pk_bf16_f32 v38, v156, v157\n"
"v_cvt_pk_bf16_f32 v39, v158, v159\n"
"global_store_dwordx4 v193, v[32:35], s[82:83] offset:0\n"
"global_store_dwordx4 v193, v[36:39], s[82:83] offset:1024\n"
"s_waitcnt lgkmcnt(8)\n"
"v_mfma_f32_32x32x16_bf16 a[112:127], v[204:207], v[184:187], a[112:127]\n"
"ds_read_b64_tr_b16 v[200:201], v220 offset:17984\n"
"ds_read_b64_tr_b16 v[202:203], v220 offset:18112\n"
"s_waitcnt lgkmcnt(8)\n"
"v_mfma_f32_32x32x16_bf16 a[112:127], v[208:211], v[188:191], a[112:127]\n"
"ds_read_b64_tr_b16 v[204:205], v220 offset:21760\n"
"ds_read_b64_tr_b16 v[206:207], v220 offset:21888\n"
"v_accvgpr_read_b32 v144, a80\n"
"v_accvgpr_read_b32 v145, a81\n"
"v_accvgpr_read_b32 v146, a82\n"
"v_accvgpr_read_b32 v147, a83\n"
"v_accvgpr_read_b32 v148, a84\n"
"v_accvgpr_read_b32 v149, a85\n"
"v_accvgpr_read_b32 v150, a86\n"
"v_accvgpr_read_b32 v151, a87\n"
"v_accvgpr_read_b32 v152, a88\n"
"v_accvgpr_read_b32 v153, a89\n"
"v_accvgpr_read_b32 v154, a90\n"
"v_accvgpr_read_b32 v155, a91\n"
"v_accvgpr_read_b32 v156, a92\n"
"v_accvgpr_read_b32 v157, a93\n"
"v_accvgpr_read_b32 v158, a94\n"
"v_accvgpr_read_b32 v159, a95\n"
"v_cvt_pk_bf16_f32 v40, v144, v145\n"
"v_cvt_pk_bf16_f32 v41, v146, v147\n"
"v_cvt_pk_bf16_f32 v42, v148, v149\n"
"v_cvt_pk_bf16_f32 v43, v150, v151\n"
"v_cvt_pk_bf16_f32 v44, v152, v153\n"
"v_cvt_pk_bf16_f32 v45, v154, v155\n"
"v_cvt_pk_bf16_f32 v46, v156, v157\n"
"v_cvt_pk_bf16_f32 v47, v158, v159\n"
"global_store_dwordx4 v193, v[40:43], s[82:83] offset:2048\n"
"global_store_dwordx4 v193, v[44:47], s[82:83] offset:3072\n"
"s_add_u32 s82, s82, 4096\n"
"s_addc_u32 s83, s83, 0\n"
"s_waitcnt lgkmcnt(8)\n"
"v_mfma_f32_32x32x16_bf16 a[128:143], v[212:215], v[184:187], a[128:143]\n"
"ds_read_b64_tr_b16 v[208:209], v220 offset:22272\n"
"ds_read_b64_tr_b16 v[210:211], v220 offset:22400\n"
"s_waitcnt lgkmcnt(8)\n"
"v_mfma_f32_32x32x16_bf16 a[128:143], v[216:219], v[188:191], a[128:143]\n"
"ds_read_b64_tr_b16 v[212:213], v220 offset:21824\n"
"ds_read_b64_tr_b16 v[214:215], v220 offset:21952\n"
"v_accvgpr_read_b32 v144, a96\n"
"v_accvgpr_read_b32 v145, a97\n"
"v_accvgpr_read_b32 v146, a98\n"
"v_accvgpr_read_b32 v147, a99\n"
"v_accvgpr_read_b32 v148, a100\n"
"v_accvgpr_read_b32 v149, a101\n"
"v_accvgpr_read_b32 v150, a102\n"
"v_accvgpr_read_b32 v151, a103\n"
"v_accvgpr_read_b32 v152, a104\n"
"v_accvgpr_read_b32 v153, a105\n"
"v_accvgpr_read_b32 v154, a106\n"
"v_accvgpr_read_b32 v155, a107\n"
"v_accvgpr_read_b32 v156, a108\n"
"v_accvgpr_read_b32 v157, a109\n"
"v_accvgpr_read_b32 v158, a110\n"
"v_accvgpr_read_b32 v159, a111\n"
"v_cvt_pk_bf16_f32 v48, v144, v145\n"
"v_cvt_pk_bf16_f32 v49, v146, v147\n"
"v_cvt_pk_bf16_f32 v50, v148, v149\n"
"v_cvt_pk_bf16_f32 v51, v150, v151\n"
"v_cvt_pk_bf16_f32 v52, v152, v153\n"
"v_cvt_pk_bf16_f32 v53, v154, v155\n"
"v_cvt_pk_bf16_f32 v54, v156, v157\n"
"v_cvt_pk_bf16_f32 v55, v158, v159\n"
"global_store_dwordx4 v193, v[48:51], s[82:83] offset:0\n"
"global_store_dwordx4 v193, v[52:55], s[82:83] offset:1024\n"
"s_waitcnt lgkmcnt(8)\n"
"v_mfma_f32_32x32x16_bf16 a[144:159], v[196:199], v[184:187], a[144:159]\n"
"ds_read_b64_tr_b16 v[216:217], v220 offset:22336\n"
"ds_read_b64_tr_b16 v[218:219], v220 offset:22464\n"
"s_waitcnt lgkmcnt(8)\n"
"v_mfma_f32_32x32x16_bf16 a[144:159], v[200:203], v[188:191], a[144:159]\n"
"ds_read_b64_tr_b16 v[196:197], v220 offset:26112\n"
"ds_read_b64_tr_b16 v[198:199], v220 offset:26240\n"
"v_accvgpr_read_b32 v144, a112\n"
"v_accvgpr_read_b32 v145, a113\n"
"v_accvgpr_read_b32 v146, a114\n"
"v_accvgpr_read_b32 v147, a115\n"
"v_accvgpr_read_b32 v148, a116\n"
"v_accvgpr_read_b32 v149, a117\n"
"v_accvgpr_read_b32 v150, a118\n"
"v_accvgpr_read_b32 v151, a119\n"
"v_accvgpr_read_b32 v152, a120\n"
"v_accvgpr_read_b32 v153, a121\n"
"v_accvgpr_read_b32 v154, a122\n"
"v_accvgpr_read_b32 v155, a123\n"
"v_accvgpr_read_b32 v156, a124\n"
"v_accvgpr_read_b32 v157, a125\n"
"v_accvgpr_read_b32 v158, a126\n"
"v_accvgpr_read_b32 v159, a127\n"
"v_cvt_pk_bf16_f32 v56, v144, v145\n"
"v_cvt_pk_bf16_f32 v57, v146, v147\n"
"v_cvt_pk_bf16_f32 v58, v148, v149\n"
"v_cvt_pk_bf16_f32 v59, v150, v151\n"
"v_cvt_pk_bf16_f32 v60, v152, v153\n"
"v_cvt_pk_bf16_f32 v61, v154, v155\n"
"v_cvt_pk_bf16_f32 v62, v156, v157\n"
"v_cvt_pk_bf16_f32 v63, v158, v159\n"
"global_store_dwordx4 v193, v[56:59], s[82:83] offset:2048\n"
"global_store_dwordx4 v193, v[60:63], s[82:83] offset:3072\n"
"s_add_u32 s82, s82, 4096\n"
"s_addc_u32 s83, s83, 0\n"
"s_waitcnt lgkmcnt(8)\n"
"v_mfma_f32_32x32x16_bf16 a[160:175], v[204:207], v[184:187], a[160:175]\n"
"ds_read_b64_tr_b16 v[200:201], v220 offset:26624\n"
"ds_read_b64_tr_b16 v[202:203], v220 offset:26752\n"
"s_waitcnt lgkmcnt(8)\n"
"v_mfma_f32_32x32x16_bf16 a[160:175], v[208:211], v[188:191], a[160:175]\n"
"ds_read_b64_tr_b16 v[204:205], v220 offset:26176\n"
"ds_read_b64_tr_b16 v[206:207], v220 offset:26304\n"
"v_accvgpr_read_b32 v144, a128\n"
"v_accvgpr_read_b32 v145, a129\n"
"v_accvgpr_read_b32 v146, a130\n"
"v_accvgpr_read_b32 v147, a131\n"
"v_accvgpr_read_b32 v148, a132\n"
"v_accvgpr_read_b32 v149, a133\n"
"v_accvgpr_read_b32 v150, a134\n"
"v_accvgpr_read_b32 v151, a135\n"
"v_accvgpr_read_b32 v152, a136\n"
"v_accvgpr_read_b32 v153, a137\n"
"v_accvgpr_read_b32 v154, a138\n"
"v_accvgpr_read_b32 v155, a139\n"
"v_accvgpr_read_b32 v156, a140\n"
"v_accvgpr_read_b32 v157, a141\n"
"v_accvgpr_read_b32 v158, a142\n"
"v_accvgpr_read_b32 v159, a143\n"
"v_cvt_pk_bf16_f32 v64, v144, v145\n"
"v_cvt_pk_bf16_f32 v65, v146, v147\n"
"v_cvt_pk_bf16_f32 v66, v148, v149\n"
"v_cvt_pk_bf16_f32 v67, v150, v151\n"
"v_cvt_pk_bf16_f32 v68, v152, v153\n"
"v_cvt_pk_bf16_f32 v69, v154, v155\n"
"v_cvt_pk_bf16_f32 v70, v156, v157\n"
"v_cvt_pk_bf16_f32 v71, v158, v159\n"
"global_store_dwordx4 v193, v[64:67], s[82:83] offset:0\n"
"global_store_dwordx4 v193, v[68:71], s[82:83] offset:1024\n"
"s_waitcnt lgkmcnt(8)\n"
"v_mfma_f32_32x32x16_bf16 a[176:191], v[212:215], v[184:187], a[176:191]\n"
"ds_read_b64_tr_b16 v[208:209], v220 offset:26688\n"
"ds_read_b64_tr_b16 v[210:211], v220 offset:26816\n"
"s_waitcnt lgkmcnt(8)\n"
"v_mfma_f32_32x32x16_bf16 a[176:191], v[216:219], v[188:191], a[176:191]\n"
"ds_read_b64_tr_b16 v[212:213], v220 offset:30464\n"
"ds_read_b64_tr_b16 v[214:215], v220 offset:30592\n"
"v_accvgpr_read_b32 v144, a144\n"
"v_accvgpr_read_b32 v145, a145\n"
"v_accvgpr_read_b32 v146, a146\n"
"v_accvgpr_read_b32 v147, a147\n"
"v_accvgpr_read_b32 v148, a148\n"
"v_accvgpr_read_b32 v149, a149\n"
"v_accvgpr_read_b32 v150, a150\n"
"v_accvgpr_read_b32 v151, a151\n"
"v_accvgpr_read_b32 v152, a152\n"
"v_accvgpr_read_b32 v153, a153\n"
"v_accvgpr_read_b32 v154, a154\n"
"v_accvgpr_read_b32 v155, a155\n"
"v_accvgpr_read_b32 v156, a156\n"
"v_accvgpr_read_b32 v157, a157\n"
"v_accvgpr_read_b32 v158, a158\n"
"v_accvgpr_read_b32 v159, a159\n"
"v_cvt_pk_bf16_f32 v72, v144, v145\n"
"v_cvt_pk_bf16_f32 v73, v146, v147\n"
"v_cvt_pk_bf16_f32 v74, v148, v149\n"
"v_cvt_pk_bf16_f32 v75, v150, v151\n"
"v_cvt_pk_bf16_f32 v76, v152, v153\n"
"v_cvt_pk_bf16_f32 v77, v154, v155\n"
"v_cvt_pk_bf16_f32 v78, v156, v157\n"
"v_cvt_pk_bf16_f32 v79, v158, v159\n"
"global_store_dwordx4 v193, v[72:75], s[82:83] offset:2048\n"
"global_store_dwordx4 v193, v[76:79], s[82:83] offset:3072\n"
"s_add_u32 s82, s82, 4096\n"
"s_addc_u32 s83, s83, 0\n"
"s_waitcnt lgkmcnt(8)\n"
"v_mfma_f32_32x32x16_bf16 a[192:207], v[196:199], v[184:187], a[192:207]\n"
"ds_read_b64_tr_b16 v[216:217], v220 offset:30976\n"
"ds_read_b64_tr_b16 v[218:219], v220 offset:31104\n"
"s_waitcnt lgkmcnt(8)\n"
"v_mfma_f32_32x32x16_bf16 a[192:207], v[200:203], v[188:191], a[192:207]\n"
"ds_read_b64_tr_b16 v[196:197], v220 offset:30528\n"
"ds_read_b64_tr_b16 v[198:199], v220 offset:30656\n"
"v_accvgpr_read_b32 v144, a160\n"
"v_accvgpr_read_b32 v145, a161\n"
"v_accvgpr_read_b32 v146, a162\n"
"v_accvgpr_read_b32 v147, a163\n"
"v_accvgpr_read_b32 v148, a164\n"
"v_accvgpr_read_b32 v149, a165\n"
"v_accvgpr_read_b32 v150, a166\n"
"v_accvgpr_read_b32 v151, a167\n"
"v_accvgpr_read_b32 v152, a168\n"
"v_accvgpr_read_b32 v153, a169\n"
"v_accvgpr_read_b32 v154, a170\n"
"v_accvgpr_read_b32 v155, a171\n"
"v_accvgpr_read_b32 v156, a172\n"
"v_accvgpr_read_b32 v157, a173\n"
"v_accvgpr_read_b32 v158, a174\n"
"v_accvgpr_read_b32 v159, a175\n"
"v_cvt_pk_bf16_f32 v80, v144, v145\n"
"v_cvt_pk_bf16_f32 v81, v146, v147\n"
"v_cvt_pk_bf16_f32 v82, v148, v149\n"
"v_cvt_pk_bf16_f32 v83, v150, v151\n"
"v_cvt_pk_bf16_f32 v84, v152, v153\n"
"v_cvt_pk_bf16_f32 v85, v154, v155\n"
"v_cvt_pk_bf16_f32 v86, v156, v157\n"
"v_cvt_pk_bf16_f32 v87, v158, v159\n"
"global_store_dwordx4 v193, v[80:83], s[82:83] offset:0\n"
"global_store_dwordx4 v193, v[84:87], s[82:83] offset:1024\n"
"s_waitcnt lgkmcnt(8)\n"
"v_mfma_f32_32x32x16_bf16 a[208:223], v[204:207], v[184:187], a[208:223]\n"
"ds_read_b64_tr_b16 v[200:201], v220 offset:31040\n"
"ds_read_b64_tr_b16 v[202:203], v220 offset:31168\n"
"s_waitcnt lgkmcnt(8)\n"
"v_mfma_f32_32x32x16_bf16 a[208:223], v[208:211], v[188:191], a[208:223]\n"
"ds_read_b64_tr_b16 v[204:205], v220 offset:34816\n"
"ds_read_b64_tr_b16 v[206:207], v220 offset:34944\n"
"v_accvgpr_read_b32 v144, a176\n"
"v_accvgpr_read_b32 v145, a177\n"
"v_accvgpr_read_b32 v146, a178\n"
"v_accvgpr_read_b32 v147, a179\n"
"v_accvgpr_read_b32 v148, a180\n"
"v_accvgpr_read_b32 v149, a181\n"
"v_accvgpr_read_b32 v150, a182\n"
"v_accvgpr_read_b32 v151, a183\n"
"v_accvgpr_read_b32 v152, a184\n"
"v_accvgpr_read_b32 v153, a185\n"
"v_accvgpr_read_b32 v154, a186\n"
"v_accvgpr_read_b32 v155, a187\n"
"v_accvgpr_read_b32 v156, a188\n"
"v_accvgpr_read_b32 v157, a189\n"
"v_accvgpr_read_b32 v158, a190\n"
"v_accvgpr_read_b32 v159, a191\n"
"v_cvt_pk_bf16_f32 v88, v144, v145\n"
"v_cvt_pk_bf16_f32 v89, v146, v147\n"
"v_cvt_pk_bf16_f32 v90, v148, v149\n"
"v_cvt_pk_bf16_f32 v91, v150, v151\n"
"v_cvt_pk_bf16_f32 v92, v152, v153\n"
"v_cvt_pk_bf16_f32 v93, v154, v155\n"
"v_cvt_pk_bf16_f32 v94, v156, v157\n"
"v_cvt_pk_bf16_f32 v95, v158, v159\n"
"global_store_dwordx4 v193, v[88:91], s[82:83] offset:2048\n"
"global_store_dwordx4 v193, v[92:95], s[82:83] offset:3072\n"
"s_add_u32 s82, s82, 4096\n"
"s_addc_u32 s83, s83, 0\n"
"s_waitcnt lgkmcnt(8)\n"
"v_mfma_f32_32x32x16_bf16 a[224:239], v[212:215], v[184:187], a[224:239]\n"
"ds_read_b64_tr_b16 v[208:209], v220 offset:35328\n"
"ds_read_b64_tr_b16 v[210:211], v220 offset:35456\n"
"s_waitcnt lgkmcnt(8)\n"
"v_mfma_f32_32x32x16_bf16 a[224:239], v[216:219], v[188:191], a[224:239]\n"
"ds_read_b64_tr_b16 v[212:213], v220 offset:34880\n"
"ds_read_b64_tr_b16 v[214:215], v220 offset:35008\n"
"v_accvgpr_read_b32 v144, a192\n"
"v_accvgpr_read_b32 v145, a193\n"
"v_accvgpr_read_b32 v146, a194\n"
"v_accvgpr_read_b32 v147, a195\n"
"v_accvgpr_read_b32 v148, a196\n"
"v_accvgpr_read_b32 v149, a197\n"
"v_accvgpr_read_b32 v150, a198\n"
"v_accvgpr_read_b32 v151, a199\n"
"v_accvgpr_read_b32 v152, a200\n"
"v_accvgpr_read_b32 v153, a201\n"
"v_accvgpr_read_b32 v154, a202\n"
"v_accvgpr_read_b32 v155, a203\n"
"v_accvgpr_read_b32 v156, a204\n"
"v_accvgpr_read_b32 v157, a205\n"
"v_accvgpr_read_b32 v158, a206\n"
"v_accvgpr_read_b32 v159, a207\n"
"v_cvt_pk_bf16_f32 v96, v144, v145\n"
"v_cvt_pk_bf16_f32 v97, v146, v147\n"
"v_cvt_pk_bf16_f32 v98, v148, v149\n"
"v_cvt_pk_bf16_f32 v99, v150, v151\n"
"v_cvt_pk_bf16_f32 v100, v152, v153\n"
"v_cvt_pk_bf16_f32 v101, v154, v155\n"
"v_cvt_pk_bf16_f32 v102, v156, v157\n"
"v_cvt_pk_bf16_f32 v103, v158, v159\n"
"global_store_dwordx4 v193, v[96:99], s[82:83] offset:0\n"
"global_store_dwordx4 v193, v[100:103], s[82:83] offset:1024\n"
"s_waitcnt lgkmcnt(8)\n"
"v_mfma_f32_32x32x16_bf16 a[240:255], v[196:199], v[184:187], a[240:255]\n"
"ds_read_b64_tr_b16 v[216:217], v220 offset:35392\n"
"ds_read_b64_tr_b16 v[218:219], v220 offset:35520\n"
"s_waitcnt lgkmcnt(8)\n"
"v_mfma_f32_32x32x16_bf16 a[240:255], v[200:203], v[188:191], a[240:255]\n"
"v_accvgpr_read_b32 v144, a208\n"
"v_accvgpr_read_b32 v145, a209\n"
"v_accvgpr_read_b32 v146, a210\n"
"v_accvgpr_read_b32 v147, a211\n"
"v_accvgpr_read_b32 v148, a212\n"
"v_accvgpr_read_b32 v149, a213\n"
"v_accvgpr_read_b32 v150, a214\n"
"v_accvgpr_read_b32 v151, a215\n"
"v_accvgpr_read_b32 v152, a216\n"
"v_accvgpr_read_b32 v153, a217\n"
"v_accvgpr_read_b32 v154, a218\n"
"v_accvgpr_read_b32 v155, a219\n"
"v_accvgpr_read_b32 v156, a220\n"
"v_accvgpr_read_b32 v157, a221\n"
"v_accvgpr_read_b32 v158, a222\n"
"v_accvgpr_read_b32 v159, a223\n"
"v_cvt_pk_bf16_f32 v104, v144, v145\n"
"v_cvt_pk_bf16_f32 v105, v146, v147\n"
"v_cvt_pk_bf16_f32 v106, v148, v149\n"
"v_cvt_pk_bf16_f32 v107, v150, v151\n"
"v_cvt_pk_bf16_f32 v108, v152, v153\n"
"v_cvt_pk_bf16_f32 v109, v154, v155\n"
"v_cvt_pk_bf16_f32 v110, v156, v157\n"
"v_cvt_pk_bf16_f32 v111, v158, v159\n"
"global_store_dwordx4 v193, v[104:107], s[82:83] offset:2048\n"
"global_store_dwordx4 v193, v[108:111], s[82:83] offset:3072\n"
"s_add_u32 s82, s82, 4096\n"
"s_addc_u32 s83, s83, 0\n"
"s_waitcnt lgkmcnt(6)\n"
"v_mfma_f32_32x32x16_bf16 v[224:239], v[204:207], v[184:187], v[224:239]\n"
"s_waitcnt lgkmcnt(4)\n"
"v_mfma_f32_32x32x16_bf16 v[224:239], v[208:211], v[188:191], v[224:239]\n"
"v_accvgpr_read_b32 v144, a224\n"
"v_accvgpr_read_b32 v145, a225\n"
"v_accvgpr_read_b32 v146, a226\n"
"v_accvgpr_read_b32 v147, a227\n"
"v_accvgpr_read_b32 v148, a228\n"
"v_accvgpr_read_b32 v149, a229\n"
"v_accvgpr_read_b32 v150, a230\n"
"v_accvgpr_read_b32 v151, a231\n"
"v_accvgpr_read_b32 v152, a232\n"
"v_accvgpr_read_b32 v153, a233\n"
"v_accvgpr_read_b32 v154, a234\n"
"v_accvgpr_read_b32 v155, a235\n"
"v_accvgpr_read_b32 v156, a236\n"
"v_accvgpr_read_b32 v157, a237\n"
"v_accvgpr_read_b32 v158, a238\n"
"v_accvgpr_read_b32 v159, a239\n"
"v_cvt_pk_bf16_f32 v112, v144, v145\n"
"v_cvt_pk_bf16_f32 v113, v146, v147\n"
"v_cvt_pk_bf16_f32 v114, v148, v149\n"
"v_cvt_pk_bf16_f32 v115, v150, v151\n"
"v_cvt_pk_bf16_f32 v116, v152, v153\n"
"v_cvt_pk_bf16_f32 v117, v154, v155\n"
"v_cvt_pk_bf16_f32 v118, v156, v157\n"
"v_cvt_pk_bf16_f32 v119, v158, v159\n"
"global_store_dwordx4 v193, v[112:115], s[82:83] offset:0\n"
"global_store_dwordx4 v193, v[116:119], s[82:83] offset:1024\n"
"s_waitcnt lgkmcnt(2)\n"
"v_mfma_f32_32x32x16_bf16 v[240:255], v[212:215], v[184:187], v[240:255]\n"
"s_waitcnt lgkmcnt(0)\n"
"v_mfma_f32_32x32x16_bf16 v[240:255], v[216:219], v[188:191], v[240:255]\n"
"v_accvgpr_read_b32 v144, a240\n"
"v_accvgpr_read_b32 v145, a241\n"
"v_accvgpr_read_b32 v146, a242\n"
"v_accvgpr_read_b32 v147, a243\n"
"v_accvgpr_read_b32 v148, a244\n"
"v_accvgpr_read_b32 v149, a245\n"
"v_accvgpr_read_b32 v150, a246\n"
"v_accvgpr_read_b32 v151, a247\n"
"v_accvgpr_read_b32 v152, a248\n"
"v_accvgpr_read_b32 v153, a249\n"
"v_accvgpr_read_b32 v154, a250\n"
"v_accvgpr_read_b32 v155, a251\n"
"v_accvgpr_read_b32 v156, a252\n"
"v_accvgpr_read_b32 v157, a253\n"
"v_accvgpr_read_b32 v158, a254\n"
"v_accvgpr_read_b32 v159, a255\n"
"v_cvt_pk_bf16_f32 v120, v144, v145\n"
"v_cvt_pk_bf16_f32 v121, v146, v147\n"
"v_cvt_pk_bf16_f32 v122, v148, v149\n"
"v_cvt_pk_bf16_f32 v123, v150, v151\n"
"v_cvt_pk_bf16_f32 v124, v152, v153\n"
"v_cvt_pk_bf16_f32 v125, v154, v155\n"
"v_cvt_pk_bf16_f32 v126, v156, v157\n"
"v_cvt_pk_bf16_f32 v127, v158, v159\n"
"global_store_dwordx4 v193, v[120:123], s[82:83] offset:2048\n"
"global_store_dwordx4 v193, v[124:127], s[82:83] offset:3072\n"
"s_add_u32 s82, s82, 4096\n"
"s_addc_u32 s83, s83, 0\n"
"s_nop 15\n"
"v_cvt_pk_bf16_f32 v128, v224, v225\n"
"v_cvt_pk_bf16_f32 v129, v226, v227\n"
"v_cvt_pk_bf16_f32 v130, v228, v229\n"
"v_cvt_pk_bf16_f32 v131, v230, v231\n"
"v_cvt_pk_bf16_f32 v132, v232, v233\n"
"v_cvt_pk_bf16_f32 v133, v234, v235\n"
"v_cvt_pk_bf16_f32 v134, v236, v237\n"
"v_cvt_pk_bf16_f32 v135, v238, v239\n"
"global_store_dwordx4 v193, v[128:131], s[82:83] offset:0\n"
"global_store_dwordx4 v193, v[132:135], s[82:83] offset:1024\n"
"v_cvt_pk_bf16_f32 v136, v240, v241\n"
"v_cvt_pk_bf16_f32 v137, v242, v243\n"
"v_cvt_pk_bf16_f32 v138, v244, v245\n"
"v_cvt_pk_bf16_f32 v139, v246, v247\n"
"v_cvt_pk_bf16_f32 v140, v248, v249\n"
"v_cvt_pk_bf16_f32 v141, v250, v251\n"
"v_cvt_pk_bf16_f32 v142, v252, v253\n"
"v_cvt_pk_bf16_f32 v143, v254, v255\n"
"global_store_dwordx4 v193, v[136:139], s[82:83] offset:2048\n"
"global_store_dwordx4 v193, v[140:143], s[82:83] offset:3072\n"
"s_waitcnt vmcnt(0)\n"
      :: [xp] "s"(xp), [pp] "s"(pp), [w1] "s"(w1), [w3] "s"(w3), [w2] "s"(w2), [wid] "s"(wid), [fs] "s"(fs), [lds] "s"(lds)
      : "memory", "scc", "vcc", VALL, AALL, S10(4), S10(5), S10(6), S10(7), S10(8), S10(9));
}
__global__ __launch_bounds__(256, 1) void k_mid(const u16* __restrict__ xnp, const u16* __restrict__ w1, const u16* __restrict__ w3, const u16* __restrict__ w2,
    const u16* __restrict__ wq, const u16* __restrict__ wk, const u16* __restrict__ wv,
    u16* __restrict__ kvf, const u16* __restrict__ wo, u16* __restrict__ P, char* __restrict__ fl) {
  __shared__ __attribute__((aligned(1024))) char smem[FFN_LDS];
  const int wid = __builtin_amdgcn_readfirstlane(threadIdx.x >> 6);
  if (blockIdx.x < 128) {
    const int xcd_ = blockIdx.x & 7, idx_ = blockIdx.x >> 3;
    const int fs = (xcd_ & 3) + 4 * (idx_ & 1), tt = (xcd_ >> 2) * 8 + (idx_ >> 1);
    ffn_body((unsigned)(size_t)(LAS char*)smem, xnp, w1, w3, w2, P, 4 * tt + wid, fs, wid);
  } else {
    attn_wg((LAS char*)smem, blockIdx.x - 128, xnp, wq, wk, wv, kvf, wo, P, fl, w1, w3, w2, wid, threadIdx.x & 63);
  }
}

__global__ __launch_bounds__(576) void k_final(const u16* __restrict__ P, const u16* __restrict__ x, u16* __restrict__ out) {
  __shared__ float tile[32][148];
  const int tb = blockIdx.x >> 2, Q = blockIdx.x & 3;
  const int b = threadIdx.x >> 6, lane = threadIdx.x & 63, hi = lane >> 5, tok = lane & 31;
  const int r = threadIdx.x / 18, c8 = threadIdx.x - r * 18;
  const size_t goff = (size_t)(tb * 32 + r) * D + Q * 144 + c8 * 8;
  u32x4 xv = *(const u32x4*)(x + goff);
  u32x4 pv[12];
#pragma unroll
  for (int s = 0; s < 12; ++s) pv[s] = *(const u32x4*)((const char*)P + ((size_t)((s * 64 + tb) * 36 + 9 * Q + b) * 64 + lane) * 16);
  float v[8];
  constexpr float IL2 = 1.f / (LOG2E * LOG2E);
#pragma unroll
  for (int j = 0; j < 8; ++j) {
    float f = 0.f;
#pragma unroll
    for (int s = 0; s < 8; ++s) { unsigned w = pv[s][j >> 1]; f += __uint_as_float((j & 1) ? (w & 0xffff0000u) : (w << 16)); }
    float sv = 0.f;
#pragma unroll
    for (int s = 8; s < 12; ++s) { unsigned w = pv[s][j >> 1]; sv += __uint_as_float((j & 1) ? (w & 0xffff0000u) : (w << 16)); }
    v[j] = sv * silu(f * IL2);
  }
  *(f32x4*)&tile[tok][16 * b + 4 * hi] = f32x4{v[0], v[1], v[2], v[3]};
  *(f32x4*)&tile[tok][16 * b + 8 + 4 * hi] = f32x4{v[4], v[5], v[6], v[7]};
  __syncthreads();
  f32x4 a0 = *(const f32x4*)&tile[r][c8 * 8], a1 = *(const f32x4*)&tile[r][c8 * 8 + 4];
  float o[8];
#pragma unroll
  for (int j = 0; j < 8; ++j) { unsigned w = xv[j >> 1]; float xx = __uint_as_float((j & 1) ? (w & 0xffff0000u) : (w << 16)); o[j] = xx + (j < 4 ? a0[j] : a1[j - 4]); }
  u32x4 ov = {cvtpk(o[0], o[1]), cvtpk(o[2], o[3]), cvtpk(o[4], o[5]), cvtpk(o[6], o[7])};
  *(u32x4*)(out + goff) = ov;
}

#define AT_T 4
#define AT_NS 4
#define AT_RG 2

extern "C" void kernel_launch(void* const* d_in, const int* in_sizes, int n_in, void* d_out, int out_size, void* d_ws, size_t ws_size, hipStream_t stream) {
  const u16* x = (const u16*)d_in[0];
  const u16* wq = (const u16*)d_in[1];
  const u16* wk = (const u16*)d_in[2];
  const u16* wv = (const u16*)d_in[3];
  const u16* wo = (const u16*)d_in[4];
  const u16* w1 = (const u16*)d_in[6];
  const u16* w3 = (const u16*)d_in[7];
  const u16* w2 = (const u16*)d_in[8];
  u16* out = (u16*)d_out;
  u16* ws = (u16*)d_ws;
  u16* P = ws;
  u16* kvf = P + 12 * S * D;
  char* fl = (char*)(kvf + 80 * 2048);
  k_mid<<<256, 256, 0, stream>>>(x, w1, w3, w2, wq, wk, wv, kvf, wo, P, fl);
  k_final<<<256, 576, 0, stream>>>(P, x, out);
}
```
